```python
import torch

def _softmax(x, axis=-1):
    return torch.softmax(x, dim=axis)
def _gelu(x):
    return torch.nn.functional.gelu(x, approximate="tanh")
def _silu(x):
    return torch.nn.functional.silu(x)
def _sigmoid(x):
    return torch.sigmoid(x)
def _layernorm(x, axis=-1, eps=1e-5):
    m = x.mean(dim=axis, keepdim=True)
    v = x.var(dim=axis, keepdim=True, unbiased=False)
    return (x - m) / torch.sqrt(v + eps)
def _scatter_add(tgt, idx, src, axis=0):
    return tgt.clone().index_add_(axis, idx, src)
def _segment_sum(src, seg, n):
    out = torch.zeros((n,) + src.shape[1:], dtype=src.dtype)
    return out.index_add_(0, seg, src)
def _pad(x, pw):
    flat = tuple(v for pair in reversed(pw) for v in pair)
    return torch.nn.functional.pad(x, flat)

kernel_name = 'sequential_preLN__spectral_fnet__moe_arch_mod__prompt'

def setup_inputs():

    _w = (lambda a: (lambda t: t.to(torch.bfloat16) if t.is_floating_point() else t)(torch.as_tensor(a)))
    def _A_setup():
        B, S, D = 1, 2048, 512
        ns = torch.arange(S)[:, None] * torch.arange(S)[None, :]
        nd = torch.arange(D)[:, None] * torch.arange(D)[None, :]
        Ws_re = torch.cos(-2.0 * torch.pi * ns / S).to(float)
        Ws_im = torch.sin(-2.0 * torch.pi * ns / S).to(float)
        Wd_re = torch.cos(-2.0 * torch.pi * nd / D).to(float)
        Wd_im = torch.sin(-2.0 * torch.pi * nd / D).to(float)
        return {
            "x": _w((lambda *s: torch.randn(*s, dtype=torch.bfloat16) * 0.1)(B, S, D)),
            "dft_seq_re": _w(Ws_re),
            "dft_seq_im": _w(Ws_im),
            "dft_hid_re": _w(Wd_re),
            "dft_hid_im": _w(Wd_im),
        }
    _A_in = _A_setup()

    def _B_setup():
        B, S, D, F, C = 1, 2048, 512, 1792, 8
        return {
            "x": _w((lambda *s: torch.randn(*s, dtype=torch.bfloat16) * 0.1)(B, S, D)),
            "w_r": _w((lambda *s: torch.randn(*s, dtype=torch.bfloat16) * 1.6)(D)),
            "w_gate": _w((lambda *s: torch.randn(*s, dtype=torch.bfloat16) * 0.8)(D, F)),
            "w_up": _w((lambda *s: torch.randn(*s, dtype=torch.bfloat16) * 0.8)(D, F)),
            "w_down": _w((lambda *s: torch.randn(*s, dtype=torch.bfloat16) * 0.8)(F, D)),
        }
    _B_in = _B_setup()

    return {
        "A_x": _A_in["x"],
        "A_dft_seq_re": _A_in["dft_seq_re"],
        "A_dft_seq_im": _A_in["dft_seq_im"],
        "A_dft_hid_re": _A_in["dft_hid_re"],
        "A_dft_hid_im": _A_in["dft_hid_im"],
        "B_x": _B_in["x"],
        "B_w_r": _B_in["w_r"],
        "B_w_gate": _B_in["w_gate"],
        "B_w_up": _B_in["w_up"],
        "B_w_down": _B_in["w_down"],
    }

def reference(A_x, A_dft_seq_re, A_dft_seq_im, A_dft_hid_re, A_dft_hid_im, B_x, B_w_r, B_w_gate, B_w_up, B_w_down):
    def _A_ref(x, dft_seq_re, dft_seq_im, dft_hid_re, dft_hid_im):
        a = torch.einsum("bsd,ed->bse", x, dft_hid_re)
        b = torch.einsum("bsd,ed->bse", x, dft_hid_im)
        y = torch.einsum("ts,bse->bte", dft_seq_re, a) - torch.einsum("ts,bse->bte", dft_seq_im, b)
        return y

    def _B_ref(x, w_r, w_gate, w_up, w_down):
        B, S, D = x.shape
        C = 8
        r = x @ w_r
        idx = torch.argsort(-r, axis=-1)[:, :C]
        idx_e = (lambda x, n, axis: torch.repeat_interleave(x, n, dim=axis))(idx.reshape(B, C, 1), D, axis=2)
        x_sel = (lambda x, i, axis: torch.take_along_dim(x, i, dim=axis))(x, idx_e, axis=1)
        g = _silu(x_sel @ w_gate)
        u = x_sel @ w_up
        y = (g * u) @ w_down
        r_sel = _sigmoid((lambda x, i, axis: torch.take_along_dim(x, i, dim=axis))(r, idx, axis=1))
        y = y * r_sel.reshape(B, C, 1)
        b_off = (torch.arange(B) * S).reshape(B, 1)
        flat_idx = (idx + b_off).reshape(B * C)
        zero = (x * 0.0).reshape(B * S, D)
        upd = _scatter_add(zero, flat_idx, y.reshape(B * C, D), axis=0)
        return x + upd.reshape(B, S, D)

    x = A_x
    h = x + _A_ref(_layernorm(x, axis=-1), A_dft_seq_re, A_dft_seq_im, A_dft_hid_re, A_dft_hid_im)
    h2 = h + _B_ref(_layernorm(h, axis=-1), B_w_r, B_w_gate, B_w_up, B_w_down)
    return h2
```

```cpp
#include <hip/hip_runtime.h>
#include <hip/hip_bf16.h>
#include <stdint.h>

typedef unsigned short u16;
typedef unsigned int u32;
typedef __attribute__((ext_vector_type(8))) short bf16x8;
typedef __attribute__((ext_vector_type(4))) short s16x4;
typedef __attribute__((ext_vector_type(4))) float f32x4;
typedef __attribute__((ext_vector_type(2))) float f32x2;
typedef __attribute__((ext_vector_type(2))) __bf16 bf16x2_t;
typedef __attribute__((ext_vector_type(4))) u32 u32x4;
typedef __attribute__((ext_vector_type(2))) u32 u32x2;

#define S_LEN 2048
#define D_DIM 512
#define F_DIM 1792

#define LDS_PTR(p) ((__attribute__((address_space(3))) void*)(p))
#define GLB_PTR(p) ((const __attribute__((address_space(1))) void*)(p))

__device__ __forceinline__ u32 pack_bf16(float lo, float hi) {
  f32x2 v = {lo, hi};
  bf16x2_t b = __builtin_convertvector(v, bf16x2_t);
  return __builtin_bit_cast(u32, b);
}
__device__ __forceinline__ float bf_lo(u32 u) { return __uint_as_float(u << 16); }
__device__ __forceinline__ float bf_hi(u32 u) { return __uint_as_float(u & 0xffff0000u); }
__device__ __forceinline__ float rbf(float x) { return bf_lo(pack_bf16(x, 0.f)); }

template <int CTRL>
__device__ __forceinline__ float dppf(float x) {
  return __builtin_bit_cast(float, __builtin_amdgcn_mov_dpp(__builtin_bit_cast(int, x), CTRL, 0xf, 0xf, true));
}
template <int CTRL>
__device__ __forceinline__ u32 dppu(u32 x) {
  return (u32)__builtin_amdgcn_mov_dpp((int)x, CTRL, 0xf, 0xf, true);
}
__device__ __forceinline__ float wave_sum(float x) {
  x += dppf<0xB1>(x);
  x += dppf<0x4E>(x);
  x += dppf<0x141>(x);
  x += dppf<0x140>(x);
  auto s = __builtin_amdgcn_permlane16_swap(__float_as_uint(x), __float_as_uint(x), false, false);
  x = __uint_as_float(s[0]) + __uint_as_float(s[1]);
  auto t = __builtin_amdgcn_permlane32_swap(__float_as_uint(x), __float_as_uint(x), false, false);
  x = __uint_as_float(t[0]) + __uint_as_float(t[1]);
  return x;
}
__device__ __forceinline__ u32 umax(u32 a, u32 b) { return a > b ? a : b; }
__device__ __forceinline__ u32 umin(u32 a, u32 b) { return a < b ? a : b; }
__device__ __forceinline__ u32 wave_max_u32(u32 x) {
  x = umax(x, dppu<0xB1>(x));
  x = umax(x, dppu<0x4E>(x));
  x = umax(x, dppu<0x141>(x));
  x = umax(x, dppu<0x140>(x));
  auto s = __builtin_amdgcn_permlane16_swap(x, x, false, false);
  x = umax(s[0], s[1]);
  auto t = __builtin_amdgcn_permlane32_swap(x, x, false, false);
  x = umax(t[0], t[1]);
  return x;
}

__global__ __launch_bounds__(512) void k1_ln_gemm1(const u16* __restrict__ x, const u16* Dr, const u16* Di,
                                                   u16* __restrict__ abt, const u16* Wsr, const u16* Wsi) {
  __shared__ __attribute__((aligned(16))) char smem[65536];
  const int b = blockIdx.x, xcd = b & 7, slot = b >> 3;
  const int sb = xcd * 4 + (slot >> 3), eb = slot & 7;
  const int tid = threadIdx.x, w = tid >> 6, l = tid & 63;
  const int c = l & 15, q = l >> 4;

  u32x4 xv[8];
  {
    const u16* xrow = x + (size_t)(sb * 64 + w * 8) * D_DIM + l * 8;
#pragma unroll
    for (int i = 0; i < 8; ++i) xv[i] = *(const u32x4*)(xrow + i * D_DIM);
  }
  bf16x8 wf[16];
  {
    const u16* W = (eb < 4 ? Dr + (size_t)eb * 128 * D_DIM : Di + (size_t)(eb - 4) * 128 * D_DIM) +
                   (size_t)(w * 16 + c) * D_DIM + q * 8;
#pragma unroll
    for (int ks = 0; ks < 16; ++ks) wf[ks] = *(const bf16x8*)(W + ks * 32);
  }
  u32 pf;
  {
    const u16* Wsx = (xcd & 1) ? Wsi : Wsr;
    const char* p = (const char*)(Wsx + (size_t)((xcd >> 1) * 512 + slot * 16) * S_LEN) + (size_t)tid * 128;
    pf = *(const u32*)p;
  }

#pragma unroll
  for (int i = 0; i < 8; ++i) {
    float f[8];
#pragma unroll
    for (int j = 0; j < 4; ++j) { f[2 * j] = bf_lo(xv[i][j]); f[2 * j + 1] = bf_hi(xv[i][j]); }
    float s = ((f[0] + f[1]) + (f[2] + f[3])) + ((f[4] + f[5]) + (f[6] + f[7]));
    s = wave_sum(s);
    const float mean = s * (1.0f / D_DIM);
    float ss = 0.f;
#pragma unroll
    for (int j = 0; j < 8; ++j) { float d = f[j] - mean; ss += d * d; }
    ss = wave_sum(ss);
    const float mb = rbf(mean);
    const float vb = rbf(ss * (1.0f / D_DIM));
    const float sd = rbf(sqrtf(rbf(vb + 1e-5f)));
    const float rinv = 1.0f / sd;
    u32x4 o;
#pragma unroll
    for (int j = 0; j < 4; ++j) {
      u32 dd = pack_bf16(f[2 * j] - mb, f[2 * j + 1] - mb);
      o[j] = pack_bf16(bf_lo(dd) * rinv, bf_hi(dd) * rinv);
    }
    const int srow = w * 8 + i;
    const int g = 4 * ((srow >> 4) & 3) + (srow & 3);
    *(u32x4*)(smem + srow * 1024 + ((l ^ g) * 16)) = o;
  }
  __syncthreads();

  f32x4 acc[4] = {};
  {
    const int rowbase = (16 * (c >> 2) + (c & 3)) * 1024;
    const int c0 = q ^ c;
#pragma unroll
    for (int ks = 0; ks < 16; ++ks) {
#pragma unroll
      for (int i = 0; i < 4; ++i) {
        const int off = rowbase + i * 4096 + (ks >> 2) * 256 + (((4 * (ks & 3)) ^ c0) * 16);
        bf16x8 a = *(const bf16x8*)(smem + off);
        acc[i] = __builtin_amdgcn_mfma_f32_16x16x32_bf16(a, wf[ks], acc[i], 0, 0, 0);
      }
    }
  }
  {
    const int E = eb * 2 + (w >> 2);
    const int p = 16 * (c >> 2) + 4 * (w & 3) + (c & 3);
    char* dst = (char*)abt + (size_t)(E * 32 + sb) * 8192 + p * 128;
    const int sw = (p >> 1) & 7;
    u32x4 o0, o1;
    o0[0] = pack_bf16(acc[0][0], acc[0][1]); o0[1] = pack_bf16(acc[0][2], acc[0][3]);
    o0[2] = pack_bf16(acc[1][0], acc[1][1]); o0[3] = pack_bf16(acc[1][2], acc[1][3]);
    o1[0] = pack_bf16(acc[2][0], acc[2][1]); o1[1] = pack_bf16(acc[2][2], acc[2][3]);
    o1[2] = pack_bf16(acc[3][0], acc[3][1]); o1[3] = pack_bf16(acc[3][2], acc[3][3]);
    *(u32x4*)(dst + (((2 * q) ^ sw) * 16)) = o0;
    *(u32x4*)(dst + (((2 * q + 1) ^ sw) * 16)) = o1;
  }
  asm volatile("" ::"v"(pf));
}

#define K2_STAGE 24576
__global__ __launch_bounds__(256) void k2_gemm2(const u16* Wsr, const u16* Wsi, const u16* abt, u16* __restrict__ PQ) {
  __shared__ __attribute__((aligned(1024))) char smem[4 * K2_STAGE];
  const int b = blockIdx.x, xcd = b & 7, slot = b >> 3;
  const int part = xcd & 1, tt = (xcd >> 1) * 4 + (slot >> 3), nt = slot & 7;
  const int t0 = tt * 128, E = part * 8 + nt;
  const int tid = threadIdx.x, w = __builtin_amdgcn_readfirstlane(tid >> 6), l = tid & 63;
  const int c = l & 15, q = l >> 4;

  const char* Abase = (const char*)((part ? Wsi : Wsr) + (size_t)t0 * S_LEN);
  const char* Bbase = (const char*)abt + (size_t)E * 32 * 8192;
  u32 avoff[4];
#pragma unroll
  for (int jj = 0; jj < 4; ++jj) {
    const int p = (w * 4 + jj) * 8 + (l >> 3);
    const int ch = (l & 7) ^ ((p >> 1) & 7);
    avoff[jj] = (u32)p * 4096u + (u32)ch * 16u;
  }
  const u32 bvoff = (u32)(w * 2) * 1024u + (u32)l * 16u;

  auto issue = [&](int kstep, int buf) {
    const char* ab = Abase + kstep * 128;
    const char* bb = Bbase + (size_t)kstep * 8192;
    char* lb = smem + buf * K2_STAGE;
#pragma unroll
    for (int jj = 0; jj < 4; ++jj)
      __builtin_amdgcn_global_load_lds(GLB_PTR(ab + avoff[jj]), LDS_PTR(lb + (w * 4 + jj) * 1024), 16, 0, 0);
#pragma unroll
    for (int jj = 0; jj < 2; ++jj)
      __builtin_amdgcn_global_load_lds(GLB_PTR(bb + bvoff + jj * 1024), LDS_PTR(lb + 16384 + (w * 2 + jj) * 1024), 16, 0, 0);
  };

  f32x4 acc[4][2] = {};
  const int sw = c >> 1;
  const int roff0 = c * 128 + ((q ^ sw) * 16);
  const int roff1 = c * 128 + (((q + 4) ^ sw) * 16);
  auto compute = [&](int buf) {
    const char* lb = smem + buf * K2_STAGE;
#pragma unroll
    for (int kk = 0; kk < 2; ++kk) {
      const int ro = kk ? roff1 : roff0;
      bf16x8 af[4], bfr[2];
#pragma unroll
      for (int j = 0; j < 2; ++j) bfr[j] = *(const bf16x8*)(lb + (w * 32 + j * 16) * 128 + ro);
#pragma unroll
      for (int i = 0; i < 4; ++i) af[i] = *(const bf16x8*)(lb + 16384 + i * 2048 + ro);
#pragma unroll
      for (int i = 0; i < 4; ++i)
#pragma unroll
        for (int j = 0; j < 2; ++j)
          acc[i][j] = __builtin_amdgcn_mfma_f32_16x16x32_bf16(af[i], bfr[j], acc[i][j], 0, 0, 0);
    }
  };
#define K2_SYNC(N) asm volatile("s_waitcnt vmcnt(" #N ") lgkmcnt(0)\n\ts_barrier" ::: "memory")

  issue(0, 0);
  issue(1, 1);
  issue(2, 2);
  for (int k = 0; k < 28; k += 4) {
    K2_SYNC(12); issue(k + 3, 3); compute(0);
    K2_SYNC(12); issue(k + 4, 0); compute(1);
    K2_SYNC(12); issue(k + 5, 1); compute(2);
    K2_SYNC(12); issue(k + 6, 2); compute(3);
  }
  K2_SYNC(12); issue(31, 3); compute(0);
  K2_SYNC(12); compute(1);
  K2_SYNC(6); compute(2);
  K2_SYNC(0); compute(3);

#pragma unroll
  for (int j = 0; j < 2; ++j) {
    const int t = t0 + w * 32 + j * 16 + c;
    u16* dst = PQ + (size_t)part * (S_LEN * D_DIM) + (size_t)t * D_DIM + nt * 64 + q * 16;
    u32x4 o0, o1;
    o0[0] = pack_bf16(acc[0][j][0], acc[0][j][1]); o0[1] = pack_bf16(acc[0][j][2], acc[0][j][3]);
    o0[2] = pack_bf16(acc[1][j][0], acc[1][j][1]); o0[3] = pack_bf16(acc[1][j][2], acc[1][j][3]);
    o1[0] = pack_bf16(acc[2][j][0], acc[2][j][1]); o1[1] = pack_bf16(acc[2][j][2], acc[2][j][3]);
    o1[2] = pack_bf16(acc[3][j][0], acc[3][j][1]); o1[3] = pack_bf16(acc[3][j][2], acc[3][j][3]);
    *(u32x4*)(dst) = o0;
    *(u32x4*)(dst + 8) = o1;
  }
}

__device__ __forceinline__ void compute_h8(const u32x4& pv, const u32x4& qv, const u32x4& xv, float* h) {
#pragma unroll
  for (int j = 0; j < 4; ++j) {
    u32 y = pack_bf16(bf_lo(pv[j]) - bf_lo(qv[j]), bf_hi(pv[j]) - bf_hi(qv[j]));
    u32 hh = pack_bf16(bf_lo(xv[j]) + bf_lo(y), bf_hi(xv[j]) + bf_hi(y));
    h[2 * j] = bf_lo(hh);
    h[2 * j + 1] = bf_hi(hh);
  }
}

__global__ __launch_bounds__(256) void k3_ln2_router(const u16* __restrict__ x, const u16* __restrict__ PQ,
                                                     const u16* __restrict__ wr, u16* __restrict__ out,
                                                     u16* __restrict__ hnbuf, u32* __restrict__ rkeys) {
  const int tid = threadIdx.x, w = tid >> 6, l = tid & 63;
  const int row = blockIdx.x * 4 + w;
  const size_t off = (size_t)row * D_DIM + l * 8;
  const u32x4 pv = *(const u32x4*)(PQ + off);
  const u32x4 qv = *(const u32x4*)(PQ + (size_t)S_LEN * D_DIM + off);
  const u32x4 xv = *(const u32x4*)(x + off);
  const u32x4 wv = *(const u32x4*)(wr + l * 8);
  float h[8];
  compute_h8(pv, qv, xv, h);
  float s = ((h[0] + h[1]) + (h[2] + h[3])) + ((h[4] + h[5]) + (h[6] + h[7]));
  s = wave_sum(s);
  const float mean = s * (1.0f / D_DIM);
  float ss = 0.f;
#pragma unroll
  for (int j = 0; j < 8; ++j) { float d = h[j] - mean; ss += d * d; }
  ss = wave_sum(ss);
  const float mb = rbf(mean);
  const float vb = rbf(ss * (1.0f / D_DIM));
  const float sd = rbf(sqrtf(rbf(vb + 1e-5f)));
  const float rinv = 1.0f / sd;
  u32x4 hn, o;
  float dot = 0.f;
#pragma unroll
  for (int j = 0; j < 4; ++j) {
    u32 dd = pack_bf16(h[2 * j] - mb, h[2 * j + 1] - mb);
    u32 n = pack_bf16(bf_lo(dd) * rinv, bf_hi(dd) * rinv);
    hn[j] = n;
    dot += bf_lo(n) * bf_lo(wv[j]);
    dot += bf_hi(n) * bf_hi(wv[j]);
    o[j] = pack_bf16(h[2 * j] + bf_lo(n), h[2 * j + 1] + bf_hi(n));
  }
  *(u32x4*)(out + off) = o;
  *(u32x4*)(hnbuf + off) = hn;
  dot = wave_sum(dot);
  if (l == 0) {
    u32 u = pack_bf16(dot, 0.f) & 0xffffu;
    u32 key = (u & 0x8000u) ? (~u & 0xffffu) : (u | 0x8000u);
    rkeys[row] = (key << 16) | (u32)(S_LEN - 1 - row);
  }
}

#define CE(a, b) { u32 _hi = umax(a, b), _lo = umin(a, b); a = _hi; b = _lo; }

__global__ __launch_bounds__(256) void k4_topk_up(const u32* __restrict__ rkeys, const u16* __restrict__ hnbuf,
                                                  const u16* wg, const u16* wu, const u16* __restrict__ x,
                                                  const u16* __restrict__ PQ, u16* __restrict__ gu,
                                                  u16* __restrict__ hnsel, u16* __restrict__ hsel,
                                                  u32* __restrict__ selinfo) {
  __shared__ __attribute__((aligned(1024))) char smem[4 * 4096 + 4 * 1024 + 256];
  char* wimg = smem;
  char* red = smem + 16384;
  u32* cand = (u32*)(smem + 16384 + 4096);
  const int b = blockIdx.x;
  const int ct = (b & 7) * 28 + (b >> 3);
  const int f0 = ct * 8;
  const int tid = threadIdx.x, w = __builtin_amdgcn_readfirstlane(tid >> 6), l = tid & 63;
  const int c = l & 15, q = l >> 4;

  const u32x4 k0 = *(const u32x4*)(rkeys + tid * 8);
  const u32x4 k1 = *(const u32x4*)(rkeys + tid * 8 + 4);
  {
    const u16* src = ((l & 1) ? wu : wg) + (size_t)(w * 128 + (l >> 1)) * F_DIM + f0;
#pragma unroll
    for (int m = 0; m < 4; ++m)
      __builtin_amdgcn_global_load_lds(GLB_PTR(src + (size_t)m * 32 * F_DIM), LDS_PTR(wimg + w * 4096 + m * 1024), 16, 0, 0);
  }
  u32 a0 = k0[0], a1 = k0[1], a2 = k0[2], a3 = k0[3], a4 = k1[0], a5 = k1[1], a6 = k1[2], a7 = k1[3];
  CE(a0, a1) CE(a2, a3) CE(a4, a5) CE(a6, a7)
  CE(a0, a2) CE(a1, a3) CE(a4, a6) CE(a5, a7)
  CE(a1, a2) CE(a5, a6) CE(a0, a4) CE(a3, a7)
  CE(a1, a5) CE(a2, a6)
  CE(a1, a4) CE(a3, a6)
  CE(a2, a4) CE(a3, a5)
  CE(a3, a4)
  u32 mycand = 0;
#pragma unroll
  for (int it = 0; it < 8; ++it) {
    const u32 m = wave_max_u32(a0);
    const bool win = (a0 == m);
    a0 = win ? a1 : a0; a1 = win ? a2 : a1; a2 = win ? a3 : a2; a3 = win ? a4 : a3;
    a4 = win ? a5 : a4; a5 = win ? a6 : a5; a6 = win ? a7 : a6; a7 = win ? 0u : a7;
    mycand = (l == it) ? m : mycand;
  }
  if (l < 8) cand[w * 8 + l] = mycand;
  __syncthreads();
  u32 selkey;
  {
    const u32 mine = cand[l & 31];
    int rank = 0;
#pragma unroll
    for (int i = 0; i < 8; ++i) {
      const u32x4 v = *(const u32x4*)(cand + i * 4);
      rank += (v[0] > mine) + (v[1] > mine) + (v[2] > mine) + (v[3] > mine);
    }
    const unsigned long long lo32 = 0xffffffffull;
    u32 res = 0;
#pragma unroll
    for (int t = 0; t < 8; ++t) {
      unsigned long long bal = __ballot(rank == t) & lo32;
      int src = __builtin_ctzll(bal);
      u32 kv = __builtin_amdgcn_readlane(mine, src);
      res = ((l & 7) == t) ? kv : res;
    }
    selkey = res;
  }
  const int myrow = S_LEN - 1 - (int)(selkey & 0xffffu);
  bf16x8 xf[4];
  {
    const u16* src = hnbuf + (size_t)myrow * D_DIM + w * 128 + q * 8;
#pragma unroll
    for (int ks = 0; ks < 4; ++ks) xf[ks] = *(const bf16x8*)(src + ks * 32);
  }
  if (b == 0) {
    if (c < 8) {
#pragma unroll
      for (int ks = 0; ks < 4; ++ks)
        *(bf16x8*)(hnsel + c * D_DIM + w * 128 + q * 8 + ks * 32) = xf[ks];
    }
    if (tid < 8) {
      const u32 key = selkey >> 16;
      const u32 u = (key & 0x8000u) ? (key & 0x7fffu) : (~key & 0xffffu);
      const float r = __uint_as_float(u << 16);
      const float sg = rbf(1.0f / (1.0f + __expf(-r)));
      selinfo[tid] = (u32)myrow;
      selinfo[8 + tid] = __float_as_uint(sg);
    }
  }
  if (b == 1) {
#pragma unroll
    for (int i = 0; i < 2; ++i) {
      const int tok = w * 2 + i;
      const int row = __builtin_amdgcn_readlane(myrow, tok);
      const size_t off = (size_t)row * D_DIM + l * 8;
      const u32x4 pv = *(const u32x4*)(PQ + off);
      const u32x4 qv = *(const u32x4*)(PQ + (size_t)S_LEN * D_DIM + off);
      const u32x4 xv = *(const u32x4*)(x + off);
      float h[8];
      compute_h8(pv, qv, xv, h);
      u32x4 o;
#pragma unroll
      for (int j = 0; j < 4; ++j) o[j] = pack_bf16(h[2 * j], h[2 * j + 1]);
      *(u32x4*)(hsel + tok * D_DIM + l * 8) = o;
    }
  }
  asm volatile("s_waitcnt vmcnt(0)" ::: "memory");
  f32x4 acc = {};
  {
    const char* base = wimg + w * 4096 + q * 256 + c * 8;
#pragma unroll
    for (int ks = 0; ks < 4; ++ks) {
      s16x4 lo = __builtin_bit_cast(s16x4, __builtin_amdgcn_ds_read_tr16_b64_v4i16(
                     (__attribute__((address_space(3))) s16x4*)(base + ks * 1024)));
      s16x4 hi = __builtin_bit_cast(s16x4, __builtin_amdgcn_ds_read_tr16_b64_v4i16(
                     (__attribute__((address_space(3))) s16x4*)(base + ks * 1024 + 128)));
      bf16x8 a = {lo[0], lo[1], lo[2], lo[3], hi[0], hi[1], hi[2], hi[3]};
      acc = __builtin_amdgcn_mfma_f32_16x16x32_bf16(a, xf[ks], acc, 0, 0, 0);
    }
  }
  *(f32x4*)(red + w * 1024 + l * 16) = acc;
  __syncthreads();
  if (w == 0 && l < 32 && c < 8) {
    f32x4 g = *(const f32x4*)(red + l * 16);
    f32x4 u = *(const f32x4*)(red + (l + 32) * 16);
#pragma unroll
    for (int ww = 1; ww < 4; ++ww) {
      g += *(const f32x4*)(red + ww * 1024 + l * 16);
      u += *(const f32x4*)(red + ww * 1024 + (l + 32) * 16);
    }
    float o[4];
#pragma unroll
    for (int r = 0; r < 4; ++r) {
      const float gp = rbf(g[r]);
      const float sg = rbf(gp / (1.0f + __expf(-gp)));
      o[r] = sg * rbf(u[r]);
    }
    u32x2 ov = {pack_bf16(o[0], o[1]), pack_bf16(o[2], o[3])};
    *(u32x2*)(gu + c * F_DIM + f0 + q * 4) = ov;
  }
}

__global__ __launch_bounds__(512) void k5_down(const u16* __restrict__ gu, const u16* wd, const u16* __restrict__ hnsel,
                                               const u16* __restrict__ hsel, const u32* __restrict__ selinfo,
                                               u16* __restrict__ out) {
  __shared__ __attribute__((aligned(1024))) char smem[8 * 7168 + 8 * 1024];
  char* wimg = smem;
  char* red = smem + 8 * 7168;
  const int b = blockIdx.x;
  const int d0 = ((b & 7) * 4 + (b >> 3)) * 16;
  const int tid = threadIdx.x, w = __builtin_amdgcn_readfirstlane(tid >> 6), l = tid & 63;
  const int c = l & 15, q = l >> 4;
  {
    const u16* src = wd + (size_t)(w * 224 + (l >> 1)) * D_DIM + d0 + (l & 1) * 8;
#pragma unroll
    for (int m = 0; m < 7; ++m)
      __builtin_amdgcn_global_load_lds(GLB_PTR(src + (size_t)m * 32 * D_DIM), LDS_PTR(wimg + w * 7168 + m * 1024), 16, 0, 0);
  }
  bf16x8 gf[7];
  {
    const u16* src = gu + (size_t)(c & 7) * F_DIM + w * 224 + q * 8;
#pragma unroll
    for (int ks = 0; ks < 7; ++ks) gf[ks] = *(const bf16x8*)(src + ks * 32);
  }
  u32x2 hnv = {0, 0}, hv = {0, 0};
  u32 row = 0;
  float sg = 0.f;
  if (w == 0) {
    hnv = *(const u32x2*)(hnsel + (c & 7) * D_DIM + d0 + q * 4);
    hv = *(const u32x2*)(hsel + (c & 7) * D_DIM + d0 + q * 4);
    row = selinfo[c & 7];
    sg = __uint_as_float(selinfo[8 + (c & 7)]);
  }
  asm volatile("s_waitcnt vmcnt(0)" ::: "memory");
  f32x4 acc = {};
  {
    const char* base = wimg + w * 7168 + q * 256 + c * 8;
#pragma unroll
    for (int ks = 0; ks < 7; ++ks) {
      s16x4 lo = __builtin_bit_cast(s16x4, __builtin_amdgcn_ds_read_tr16_b64_v4i16(
                     (__attribute__((address_space(3))) s16x4*)(base + ks * 1024)));
      s16x4 hi = __builtin_bit_cast(s16x4, __builtin_amdgcn_ds_read_tr16_b64_v4i16(
                     (__attribute__((address_space(3))) s16x4*)(base + ks * 1024 + 128)));
      bf16x8 a = {lo[0], lo[1], lo[2], lo[3], hi[0], hi[1], hi[2], hi[3]};
      acc = __builtin_amdgcn_mfma_f32_16x16x32_bf16(a, gf[ks], acc, 0, 0, 0);
    }
  }
  *(f32x4*)(red + w * 1024 + l * 16) = acc;
  __syncthreads();
  if (w == 0 && c < 8) {
    f32x4 y = *(const f32x4*)(red + l * 16);
#pragma unroll
    for (int ww = 1; ww < 8; ++ww) y += *(const f32x4*)(red + ww * 1024 + l * 16);
    float hn4[4] = {bf_lo(hnv[0]), bf_hi(hnv[0]), bf_lo(hnv[1]), bf_hi(hnv[1])};
    float h4[4] = {bf_lo(hv[0]), bf_hi(hv[0]), bf_lo(hv[1]), bf_hi(hv[1])};
    float o[4];
#pragma unroll
    for (int r = 0; r < 4; ++r) {
      const float yy = rbf(rbf(y[r]) * sg);
      o[r] = h4[r] + rbf(hn4[r] + yy);
    }
    u32x2 ov = {pack_bf16(o[0], o[1]), pack_bf16(o[2], o[3])};
    *(u32x2*)(out + (size_t)row * D_DIM + d0 + q * 4) = ov;
  }
}

extern "C" void kernel_launch(void* const* d_in, const int* in_sizes, int n_in, void* d_out, int out_size, void* d_ws,
                              size_t ws_size, hipStream_t stream) {
  const u16* x = (const u16*)d_in[0];
  const u16* Wsr = (const u16*)d_in[1];
  const u16* Wsi = (const u16*)d_in[2];
  const u16* Dr = (const u16*)d_in[3];
  const u16* Di = (const u16*)d_in[4];
  const u16* wr = (const u16*)d_in[6];
  const u16* wg = (const u16*)d_in[7];
  const u16* wu = (const u16*)d_in[8];
  const u16* wd = (const u16*)d_in[9];
  u16* out = (u16*)d_out;
  char* ws = (char*)d_ws;
  u16* abt = (u16*)(ws);
  u16* PQ = (u16*)(ws + (4u << 20));
  u16* hnbuf = (u16*)(ws + (8u << 20));
  u32* rkeys = (u32*)(ws + (10u << 20));
  u16* gu = (u16*)(ws + (10u << 20) + 65536);
  u16* hnsel = (u16*)(ws + (10u << 20) + 131072);
  u16* hsel = (u16*)(ws + (10u << 20) + 196608);
  u32* selinfo = (u32*)(ws + (10u << 20) + 262144);

  k1_ln_gemm1<<<256, 512, 0, stream>>>(x, Dr, Di, abt, Wsr, Wsi);
  k2_gemm2<<<256, 256, 0, stream>>>(Wsr, Wsi, abt, PQ);
  k3_ln2_router<<<512, 256, 0, stream>>>(x, PQ, wr, out, hnbuf, rkeys);
  k4_topk_up<<<224, 256, 0, stream>>>(rkeys, hnbuf, wg, wu, x, PQ, gu, hnsel, hsel, selinfo);
  k5_down<<<32, 512, 0, stream>>>(gu, wd, hnsel, hsel, selinfo, out);
}
```

```cpp
#include <hip/hip_runtime.h>
#include <hip/hip_bf16.h>
#include <stdint.h>

typedef unsigned short u16;
typedef unsigned int u32;
typedef __attribute__((ext_vector_type(8))) short bf16x8;
typedef __attribute__((ext_vector_type(4))) short s16x4;
typedef __attribute__((ext_vector_type(4))) float f32x4;
typedef __attribute__((ext_vector_type(2))) float f32x2;
typedef __attribute__((ext_vector_type(2))) __bf16 bf16x2_t;
typedef __attribute__((ext_vector_type(4))) u32 u32x4;
typedef __attribute__((ext_vector_type(2))) u32 u32x2;

#define STAMP(K,I)

#define S_LEN 2048
#define D_DIM 512
#define F_DIM 1792

#define LDS_PTR(p) ((__attribute__((address_space(3))) void*)(p))
#define GLB_PTR(p) ((const __attribute__((address_space(1))) void*)(p))
#define SB() __builtin_amdgcn_sched_barrier(0)
#define CBAR() asm volatile("" ::: "memory")
#define IL1() __builtin_amdgcn_sched_group_barrier(0x008, 1, 0); __builtin_amdgcn_sched_group_barrier(0x100, 1, 0);
#define IL() do { IL1() IL1() IL1() IL1() IL1() IL1() IL1() IL1() __builtin_amdgcn_sched_group_barrier(0x008, 8, 0); } while (0)
#define LDS_BARRIER() asm volatile("s_waitcnt lgkmcnt(0)\n\ts_barrier" ::: "memory")

__device__ __forceinline__ u32 pack_bf16(float lo, float hi) {
  f32x2 v = {lo, hi};
  bf16x2_t b = __builtin_convertvector(v, bf16x2_t);
  return __builtin_bit_cast(u32, b);
}
__device__ __forceinline__ float bf_lo(u32 u) { return __uint_as_float(u << 16); }
__device__ __forceinline__ float bf_hi(u32 u) { return __uint_as_float(u & 0xffff0000u); }
__device__ __forceinline__ float rbf(float x) { return bf_lo(pack_bf16(x, 0.f)); }
__device__ __forceinline__ float dot2bf(u32 a, u32 b, float acc) {
  return __builtin_amdgcn_fdot2_f32_bf16(__builtin_bit_cast(bf16x2_t, a), __builtin_bit_cast(bf16x2_t, b), acc, false);
}

template <int CTRL>
__device__ __forceinline__ float dppf(float x) {
  return __builtin_bit_cast(float, __builtin_amdgcn_mov_dpp(__builtin_bit_cast(int, x), CTRL, 0xf, 0xf, true));
}
template <int CTRL>
__device__ __forceinline__ u32 dppu(u32 x) {
  return (u32)__builtin_amdgcn_mov_dpp((int)x, CTRL, 0xf, 0xf, true);
}
__device__ __forceinline__ float xrow_sum(float x) {
  auto s = __builtin_amdgcn_permlane16_swap(__float_as_uint(x), __float_as_uint(x), false, false);
  x = __uint_as_float(s[0]) + __uint_as_float(s[1]);
  auto t = __builtin_amdgcn_permlane32_swap(__float_as_uint(x), __float_as_uint(x), false, false);
  return __uint_as_float(t[0]) + __uint_as_float(t[1]);
}
__device__ __forceinline__ float wave_sum(float x) {
  x += dppf<0xB1>(x);
  x += dppf<0x4E>(x);
  x += dppf<0x141>(x);
  x += dppf<0x140>(x);
  return xrow_sum(x);
}
__device__ __forceinline__ u32 umax(u32 a, u32 b) { return a > b ? a : b; }
__device__ __forceinline__ u32 umin(u32 a, u32 b) { return a < b ? a : b; }
__device__ __forceinline__ u32 wave_max_u32(u32 x) {
  x = umax(x, dppu<0xB1>(x));
  x = umax(x, dppu<0x4E>(x));
  x = umax(x, dppu<0x141>(x));
  x = umax(x, dppu<0x140>(x));
  auto s = __builtin_amdgcn_permlane16_swap(x, x, false, false);
  x = umax(s[0], s[1]);
  auto t = __builtin_amdgcn_permlane32_swap(x, x, false, false);
  x = umax(t[0], t[1]);
  return x;
}
__device__ __forceinline__ void ln_scalars(float s1, float s2, float& mb, float& rinv) {
  const float mean = s1 * (1.0f / D_DIM);
  const float var = fmaxf(s2 * (1.0f / D_DIM) - mean * mean, 0.f);
  mb = rbf(mean);
  const float vb = rbf(var);
  const float sd = rbf(__builtin_amdgcn_sqrtf(rbf(vb + 1e-5f)));
  rinv = __builtin_amdgcn_rcpf(sd);
}

__global__ __launch_bounds__(512, 2) void k1_ln_gemm1(const u16* x, const u16* Dr, const u16* Di,
                                                      u16* __restrict__ abt, const u16* Wsr, const u16* Wsi) {
  __shared__ __attribute__((aligned(1024))) char smem[65536];
  const int b = blockIdx.x, xcd = b & 7, slot = b >> 3;
  const int Qd = xcd & 3, part = Qd >> 1, sh = Qd & 1;
  const int nt = (xcd >> 2) * 2 + (slot >> 4), ks0 = slot & 15;
  const int s0 = sh * 1024 + ks0 * 64;
  const int tid = threadIdx.x, w = __builtin_amdgcn_readfirstlane(tid >> 6), l = tid & 63;
  const int c = l & 15, q = l >> 4;
  const u16* Wp = part ? Di : Dr;
  STAMP(0,0);
  u32 pf0 = 0, pf1;
  if (w == 0) {
    const char* p = (const char*)(Wp + (size_t)(xcd >> 2) * 256 * D_DIM) + (size_t)slot * 8192 + l * 128;
    pf0 = *(const u32*)p;
  }
  CBAR();
  u32x4 xv[8];
  {
    const u16* xrow = x + (size_t)(s0 + w * 8) * D_DIM + l * 8;
#pragma unroll
    for (int i = 0; i < 8; ++i) xv[i] = *(const u32x4*)(xrow + i * D_DIM);
  }
  CBAR();
  bf16x8 wf[16];
  {
    const u16* W = Wp + (size_t)(nt * 128 + w * 16 + c) * D_DIM + q * 8;
#pragma unroll
    for (int ks = 0; ks < 16; ++ks) wf[ks] = *(const bf16x8*)(W + ks * 32);
  }
  CBAR();
  {
    const u16* Wsx = part ? Wsi : Wsr;
    const char* p = (const char*)(Wsx + (size_t)((xcd >> 2) * 1024 + slot * 32 + (tid >> 4)) * S_LEN + sh * 1024) + (tid & 15) * 128;
    pf1 = __builtin_nontemporal_load((const u32*)p);
  }
  CBAR();
  STAMP(0,1);
  float s1[8], s2[8];
#pragma unroll
  for (int i = 0; i < 8; ++i) {
    float a = 0.f, bb = 0.f;
#pragma unroll
    for (int j = 0; j < 4; ++j) { a = dot2bf(xv[i][j], 0x3f803f80u, a); bb = dot2bf(xv[i][j], xv[i][j], bb); }
    s1[i] = a; s2[i] = bb;
  }
  {
    const bool o1 = l & 1, o2 = l & 2;
    float r1[4], r2[4];
#pragma unroll
    for (int a = 0; a < 4; ++a) {
      float t = o1 ? s1[2 * a + 1] : s1[2 * a], o = o1 ? s1[2 * a] : s1[2 * a + 1];
      r1[a] = t + dppf<0xB1>(o);
      t = o1 ? s2[2 * a + 1] : s2[2 * a]; o = o1 ? s2[2 * a] : s2[2 * a + 1];
      r2[a] = t + dppf<0xB1>(o);
    }
    float u1[2], u2[2];
#pragma unroll
    for (int m = 0; m < 2; ++m) {
      float t = o2 ? r1[2 * m + 1] : r1[2 * m], o = o2 ? r1[2 * m] : r1[2 * m + 1];
      u1[m] = t + dppf<0x4E>(o);
      t = o2 ? r2[2 * m + 1] : r2[2 * m]; o = o2 ? r2[2 * m] : r2[2 * m + 1];
      u2[m] = t + dppf<0x4E>(o);
    }
#pragma unroll
    for (int m = 0; m < 2; ++m) {
      u1[m] += dppf<0x124>(u1[m]); u2[m] += dppf<0x124>(u2[m]);
      u1[m] += dppf<0x128>(u1[m]); u2[m] += dppf<0x128>(u2[m]);
      u1[m] = xrow_sum(u1[m]); u2[m] = xrow_sum(u2[m]);
    }
    float mbv[2], riv[2];
#pragma unroll
    for (int m = 0; m < 2; ++m) ln_scalars(u1[m], u2[m], mbv[m], riv[m]);
#pragma unroll
    for (int i = 0; i < 8; ++i) {
      float mb, ri;
      switch (i & 3) {
        case 0: mb = dppf<0x00>(mbv[i >> 2]); ri = dppf<0x00>(riv[i >> 2]); break;
        case 1: mb = dppf<0x55>(mbv[i >> 2]); ri = dppf<0x55>(riv[i >> 2]); break;
        case 2: mb = dppf<0xAA>(mbv[i >> 2]); ri = dppf<0xAA>(riv[i >> 2]); break;
        default: mb = dppf<0xFF>(mbv[i >> 2]); ri = dppf<0xFF>(riv[i >> 2]); break;
      }
      u32x4 o;
#pragma unroll
      for (int j = 0; j < 4; ++j) {
        u32 dd = pack_bf16(bf_lo(xv[i][j]) - mb, bf_hi(xv[i][j]) - mb);
        o[j] = pack_bf16(bf_lo(dd) * ri, bf_hi(dd) * ri);
      }
      const int srow = w * 8 + i;
      const int g = 4 * ((srow >> 4) & 3) + (srow & 3);
      *(u32x4*)(smem + srow * 1024 + ((l ^ g) * 16)) = o;
    }
  }
  STAMP(0,2);
  LDS_BARRIER();
  STAMP(0,3);

  f32x4 acc[4] = {};
  {
    const int rowbase = (16 * (c >> 2) + (c & 3)) * 1024;
    const int c0 = q ^ c;
    bf16x8 af[3][4];
    auto rdA = [&](int ks) {
#pragma unroll
      for (int i = 0; i < 4; ++i)
        af[ks % 3][i] = *(const bf16x8*)(smem + rowbase + i * 4096 + (ks >> 2) * 256 + (((4 * (ks & 3)) ^ c0) * 16));
    };
    rdA(0);
    rdA(1);
    SB();
#pragma unroll
    for (int ks = 0; ks < 16; ++ks) {
      if (ks + 2 < 16) rdA(ks + 2);
#pragma unroll
      for (int i = 0; i < 4; ++i)
        acc[i] = __builtin_amdgcn_mfma_f32_16x16x32_bf16(af[ks % 3][i], wf[ks], acc[i], 0, 0, 0);
      if (ks + 2 < 16) { IL1() IL1() IL1() IL1() }
      SB();
    }
  }
  STAMP(0,4);
  {
    const int e6 = (w & 3) * 16 + c;
    const int ii = ((e6 >> 5) << 1) | ((e6 >> 2) & 1), qq = (e6 >> 3) & 3, rr = e6 & 3;
    const int p = (w >> 2) * 64 + 16 * ii + 4 * qq + rr;
    char* dst = (char*)abt + (size_t)((Qd * 4 + nt) * 16 + ks0) * 16384 + p * 128;
    const int sw = (p >> 1) & 7;
    u32x4 o0, o1;
    o0[0] = pack_bf16(acc[0][0], acc[0][1]); o0[1] = pack_bf16(acc[0][2], acc[0][3]);
    o0[2] = pack_bf16(acc[1][0], acc[1][1]); o0[3] = pack_bf16(acc[1][2], acc[1][3]);
    o1[0] = pack_bf16(acc[2][0], acc[2][1]); o1[1] = pack_bf16(acc[2][2], acc[2][3]);
    o1[2] = pack_bf16(acc[3][0], acc[3][1]); o1[3] = pack_bf16(acc[3][2], acc[3][3]);
    *(u32x4*)(dst + (((2 * q) ^ sw) * 16)) = o0;
    *(u32x4*)(dst + (((2 * q + 1) ^ sw) * 16)) = o1;
  }
  STAMP(0,5);
  asm volatile("" ::"v"(pf0), "v"(pf1));
  STAMP(0,6);
}

#define K2_STG 32768
#define K2_NST 4
template <int N> __device__ __forceinline__ void k2_waitbar() { asm volatile("s_waitcnt vmcnt(%0)\n\ts_barrier" ::"n"(N) : "memory"); }

__global__ __launch_bounds__(512, 2) void k2_gemm2(const u16* Wsr, const u16* Wsi, const u16* abt, u16* __restrict__ PQ,
                                                   const u16* wg, const u16* wu, const u16* wd,
                                                   u16* __restrict__ wgu_t, u16* __restrict__ wd_t) {
  __shared__ __attribute__((aligned(1024))) char smem[K2_NST * K2_STG];
  const int b = blockIdx.x, xcd = b & 7, slot = b >> 3;
  const int Qd = xcd & 3, part = Qd >> 1, sh = Qd & 1;
  const int mt = slot >> 2, nt = slot & 3;
  const int t0 = ((xcd >> 2) * 8 + mt) * 128;
  const int tid = threadIdx.x, w = __builtin_amdgcn_readfirstlane(tid >> 6), l = tid & 63;
  const int c = l & 15, q = l >> 4;
  constexpr int D = K2_NST - 1;
  const int k0 = slot & 15;
  STAMP(1,0);
  if (w >= 4) {
    const int lw = w - 4;
    const char* Abase = (const char*)((part ? Wsi : Wsr) + (size_t)t0 * S_LEN + sh * 1024);
    const char* Bbase = (const char*)abt + (size_t)(Qd * 4 + nt) * 16 * 16384;
    const u32 av0 = (u32)(l >> 3) * 4096u + (u32)((l & 7) ^ (l >> 4)) * 16u;
    const u32 av1 = (u32)(l >> 3) * 4096u + (u32)((l & 7) ^ (4 + (l >> 4))) * 16u;
    const u32 bv = (u32)l * 16u;
    auto issue = [&](int k, int buf) {
      const int kstep = (k + k0) & 15;
      const char* ab = Abase + kstep * 128;
      const char* bb = Bbase + (size_t)kstep * 16384;
      char* lb = smem + buf * K2_STG;
#pragma unroll
      for (int jj = 0; jj < 4; ++jj) {
        const int ja = lw * 4 + jj;
        __builtin_amdgcn_global_load_lds(GLB_PTR(bb + ja * 1024 + bv), LDS_PTR(lb + 16384 + ja * 1024), 16, 0, 0);
        __builtin_amdgcn_global_load_lds(GLB_PTR(ab + (size_t)ja * 32768 + ((jj & 1) ? av1 : av0)), LDS_PTR(lb + ja * 1024), 16, 0, 0);
      }
    };
#pragma unroll 1
    for (int s = 0; s < D; ++s) issue(s, s);
    int nbuf = D;
#pragma unroll 1
    for (int k = 0; k < 16 - D; ++k) {
      k2_waitbar<(D - 1) * 8>();
      issue(k + D, nbuf);
      nbuf = (nbuf + 1 == K2_NST) ? 0 : nbuf + 1;
    }
    const int lt = tid - 256;
    u32x4 rv[6];
    int dsto[6];
#pragma unroll
    for (int i = 0; i < 4; ++i) {
      int idx = lt + i * 256;
      if (idx > 895) idx = 895;
      const int ch = idx % 28, rm = idx / 28, mat = rm & 1, r = slot * 16 + (rm >> 1);
      const u16* src = (mat ? wu : wg) + (size_t)r * F_DIM + xcd * 224 + ch * 8;
      rv[i] = __builtin_nontemporal_load((const u32x4*)src);
      dsto[i] = (((xcd * 28 + ch) * 512 + r) * 2 + mat) * 8;
    }
#pragma unroll
    for (int i = 0; i < 2; ++i) {
      int idx = lt + i * 256;
      if (idx > 447) idx = 447;
      const int ch = idx & 7, r = slot * 56 + (idx >> 3);
      const u16* src = wd + (size_t)r * D_DIM + xcd * 64 + ch * 8;
      rv[4 + i] = __builtin_nontemporal_load((const u32x4*)src);
      dsto[4 + i] = ((xcd * 4 + (ch >> 1)) * F_DIM + r) * 16 + (ch & 1) * 8;
    }
    CBAR();
    k2_waitbar<2 * 8 + 6>();
    k2_waitbar<1 * 8 + 6>();
    k2_waitbar<6>();
#pragma unroll
    for (int i = 0; i < 4; ++i) *(u32x4*)(wgu_t + dsto[i]) = rv[i];
#pragma unroll
    for (int i = 0; i < 2; ++i) *(u32x4*)(wd_t + dsto[4 + i]) = rv[4 + i];
    STAMP(1,1); STAMP(1,2); STAMP(1,3);
    return;
  }
  const int we = w & 1, wt = w >> 1;
  f32x4 acc[4][4] = {};
  const int sw = c >> 1;
  const int ro0 = c * 128 + ((q ^ sw) * 16);
  const int ro1 = c * 128 + (((q + 4) ^ sw) * 16);
  const int aoff = 16384 + we * 8192, boff = wt * 8192;
  bf16x8 a0[4], b0[4], a1[4], b1[4];
  auto rd = [&](bf16x8* af, bf16x8* bf, int buf, int ro) {
    const char* lb = smem + buf * K2_STG + ro;
#pragma unroll
    for (int i = 0; i < 4; ++i) { af[i] = *(const bf16x8*)(lb + aoff + i * 2048); bf[i] = *(const bf16x8*)(lb + boff + i * 2048); }
  };
  auto mm = [&](const bf16x8* af, const bf16x8* bf) {
#pragma unroll
    for (int i = 0; i < 4; ++i)
#pragma unroll
      for (int j = 0; j < 4; ++j)
        acc[i][j] = __builtin_amdgcn_mfma_f32_16x16x32_bf16(af[i], bf[j], acc[i][j], 0, 0, 0);
  };
  asm volatile("s_barrier" ::: "memory");
  STAMP(1,1);
  {
    int buf = 0;
    rd(a0, b0, 0, ro0);
#pragma unroll 1
    for (int k = 0; k < 15; ++k) {
      rd(a1, b1, buf, ro1);
      mm(a0, b0);
      IL();
      SB();
      LDS_BARRIER();
      buf = (buf + 1 == K2_NST) ? 0 : buf + 1;
      rd(a0, b0, buf, ro0);
      mm(a1, b1);
      IL();
      SB();
    }
    rd(a1, b1, buf, ro1);
    mm(a0, b0);
    IL();
    SB();
    mm(a1, b1);
  }
  STAMP(1,2);
#pragma unroll
  for (int j = 0; j < 4; ++j) {
    const int t = t0 + wt * 64 + j * 16 + c;
    u16* dst = PQ + (size_t)Qd * (S_LEN * D_DIM) + (size_t)t * D_DIM + nt * 128 + we * 64 + q * 8;
    u32x4 o0, o1;
    o0[0] = pack_bf16(acc[0][j][0], acc[0][j][1]); o0[1] = pack_bf16(acc[0][j][2], acc[0][j][3]);
    o0[2] = pack_bf16(acc[1][j][0], acc[1][j][1]); o0[3] = pack_bf16(acc[1][j][2], acc[1][j][3]);
    o1[0] = pack_bf16(acc[2][j][0], acc[2][j][1]); o1[1] = pack_bf16(acc[2][j][2], acc[2][j][3]);
    o1[2] = pack_bf16(acc[3][j][0], acc[3][j][1]); o1[3] = pack_bf16(acc[3][j][2], acc[3][j][3]);
    *(u32x4*)(dst) = o0;
    *(u32x4*)(dst + 32) = o1;
  }
  STAMP(1,3);
}

__device__ __forceinline__ u32x4 compute_h8(const u32x4& p0, const u32x4& p1, const u32x4& q0, const u32x4& q1, const u32x4& xv) {
  u32x4 h;
#pragma unroll
  for (int j = 0; j < 4; ++j) {
    u32 p = pack_bf16(bf_lo(p0[j]) + bf_lo(p1[j]), bf_hi(p0[j]) + bf_hi(p1[j]));
    u32 qq = pack_bf16(bf_lo(q0[j]) + bf_lo(q1[j]), bf_hi(q0[j]) + bf_hi(q1[j]));
    u32 y = pack_bf16(bf_lo(p) - bf_lo(qq), bf_hi(p) - bf_hi(qq));
    h[j] = pack_bf16(bf_lo(xv[j]) + bf_lo(y), bf_hi(xv[j]) + bf_hi(y));
  }
  return h;
}

__global__ __launch_bounds__(512) void k3_ln2_router(const u16* __restrict__ x, const u16* __restrict__ PQ,
                                                     const u16* __restrict__ wr, u16* __restrict__ out,
                                                     u16* __restrict__ hnbuf, u16* __restrict__ hbuf, u32* __restrict__ rkeys) {
  const int tid = threadIdx.x, w = tid >> 6, l = tid & 63;
  const int b = blockIdx.x, xcd = b & 7, slot = b >> 3;
  const int row = xcd * 256 + slot * 8 + w;
  STAMP(2,0);
  const size_t off = (size_t)row * D_DIM + l * 8;
  const size_t PS = (size_t)S_LEN * D_DIM;
  const u32x4 p0 = *(const u32x4*)(PQ + off);
  const u32x4 p1 = *(const u32x4*)(PQ + PS + off);
  const u32x4 q0 = *(const u32x4*)(PQ + 2 * PS + off);
  const u32x4 q1 = *(const u32x4*)(PQ + 3 * PS + off);
  const u32x4 xv = *(const u32x4*)(x + off);
  const u32x4 wv = *(const u32x4*)(wr + l * 8);
  const u32x4 h = compute_h8(p0, p1, q0, q1, xv);
  STAMP(2,1);
  float s1 = 0.f, s2 = 0.f;
#pragma unroll
  for (int j = 0; j < 4; ++j) { s1 = dot2bf(h[j], 0x3f803f80u, s1); s2 = dot2bf(h[j], h[j], s2); }
  *(u32x4*)(hbuf + off) = h;
  s1 = wave_sum(s1);
  s2 = wave_sum(s2);
  float mb, rinv;
  ln_scalars(s1, s2, mb, rinv);
  u32x4 hn, o;
  float dot = 0.f;
#pragma unroll
  for (int j = 0; j < 4; ++j) {
    u32 dd = pack_bf16(bf_lo(h[j]) - mb, bf_hi(h[j]) - mb);
    u32 n = pack_bf16(bf_lo(dd) * rinv, bf_hi(dd) * rinv);
    hn[j] = n;
    dot = dot2bf(n, wv[j], dot);
    o[j] = pack_bf16(bf_lo(h[j]) + bf_lo(n), bf_hi(h[j]) + bf_hi(n));
  }
  *(u32x4*)(out + off) = o;
  *(u32x4*)(hnbuf + off) = hn;
  dot = wave_sum(dot);
  if (l == 0) {
    u32 u = pack_bf16(dot, 0.f) & 0xffffu;
    u32 key = (u & 0x8000u) ? (~u & 0xffffu) : (u | 0x8000u);
    rkeys[row] = (key << 16) | (u32)(S_LEN - 1 - row);
  }
  STAMP(2,2);
}

#define CE(a, b) { u32 _hi = umax(a, b), _lo = umin(a, b); a = _hi; b = _lo; }

__global__ __launch_bounds__(256) void k4_topk_up(const u32* __restrict__ rkeys, const u16* __restrict__ hnbuf,
                                                  const u16* wgu_t, const u16* __restrict__ hbuf, u16* __restrict__ gu,
                                                  u16* __restrict__ hnsel, u16* __restrict__ hsel,
                                                  u32* __restrict__ selinfo) {
  __shared__ __attribute__((aligned(1024))) char smem[4 * 4096 + 4 * 1024 + 256];
  char* wimg = smem;
  char* red = smem + 16384;
  u32* cand = (u32*)(smem + 16384 + 4096);
  const int b = blockIdx.x;
  const int ct = (b & 7) * 28 + (b >> 3);
  const int f0 = ct * 8;
  const int tid = threadIdx.x, w = __builtin_amdgcn_readfirstlane(tid >> 6), l = tid & 63;
  const int c = l & 15, q = l >> 4;
  STAMP(3,0);
  const u32x4 k0 = *(const u32x4*)(rkeys + tid * 8);
  const u32x4 k1 = *(const u32x4*)(rkeys + tid * 8 + 4);
  CBAR();
  u32x4 wreg[4];
  {
    const char* src = (const char*)wgu_t + (size_t)ct * 16384 + w * 4096 + l * 16;
#pragma unroll
    for (int m = 0; m < 4; ++m) wreg[m] = *(const u32x4*)(src + m * 1024);
  }
  CBAR();
  u32 a0 = k0[0], a1 = k0[1], a2 = k0[2], a3 = k0[3], a4 = k1[0], a5 = k1[1], a6 = k1[2], a7 = k1[3];
  STAMP(3,1);
  CE(a0, a1) CE(a2, a3) CE(a4, a5) CE(a6, a7)
  CE(a0, a2) CE(a1, a3) CE(a4, a6) CE(a5, a7)
  CE(a1, a2) CE(a5, a6) CE(a0, a4) CE(a3, a7)
  CE(a1, a5) CE(a2, a6)
  CE(a1, a4) CE(a3, a6)
  CE(a2, a4) CE(a3, a5)
  CE(a3, a4)
  u32 mycand = 0;
#pragma unroll
  for (int it = 0; it < 8; ++it) {
    const u32 m = wave_max_u32(a0);
    const bool win = (a0 == m);
    a0 = win ? a1 : a0; a1 = win ? a2 : a1; a2 = win ? a3 : a2; a3 = win ? a4 : a3;
    a4 = win ? a5 : a4; a5 = win ? a6 : a5; a6 = win ? a7 : a6; a7 = win ? 0u : a7;
    mycand = (l == it) ? m : mycand;
  }
  if (l < 8) cand[w * 8 + l] = mycand;
  LDS_BARRIER();
  u32 selkey;
  {
    const u32 mine = cand[l & 31];
    int rank = 0;
#pragma unroll
    for (int i = 0; i < 8; ++i) {
      const u32x4 v = *(const u32x4*)(cand + i * 4);
      rank += (v[0] > mine) + (v[1] > mine) + (v[2] > mine) + (v[3] > mine);
    }
    u32 res = 0;
#pragma unroll
    for (int t = 0; t < 8; ++t) {
      unsigned long long bal = __ballot(rank == t) & 0xffffffffull;
      int src = __builtin_ctzll(bal);
      u32 kv = __builtin_amdgcn_readlane(mine, src);
      res = ((l & 7) == t) ? kv : res;
    }
    selkey = res;
  }
  STAMP(3,2);
  const int myrow = S_LEN - 1 - (int)(selkey & 0xffffu);
  bf16x8 xf[4];
  {
    const u16* src = hnbuf + (size_t)myrow * D_DIM + w * 128 + q * 8;
#pragma unroll
    for (int ks = 0; ks < 4; ++ks) xf[ks] = *(const bf16x8*)(src + ks * 32);
  }
  CBAR();
#pragma unroll
  for (int m = 0; m < 4; ++m) *(u32x4*)(wimg + w * 4096 + m * 1024 + l * 16) = wreg[m];
  if (b == 0) {
    if (c < 8) {
#pragma unroll
      for (int ks = 0; ks < 4; ++ks)
        *(bf16x8*)(hnsel + c * D_DIM + w * 128 + q * 8 + ks * 32) = xf[ks];
    }
    if (tid < 8) {
      const u32 key = selkey >> 16;
      const u32 u = (key & 0x8000u) ? (key & 0x7fffu) : (~key & 0xffffu);
      const float r = __uint_as_float(u << 16);
      const float sg = rbf(1.0f / (1.0f + __expf(-r)));
      selinfo[tid] = (u32)myrow;
      selinfo[8 + tid] = __float_as_uint(sg);
    }
  }
  if (b == 1) {
    if (c < 8) {
      const u16* src = hbuf + (size_t)myrow * D_DIM + w * 128 + q * 8;
#pragma unroll
      for (int ks = 0; ks < 4; ++ks)
        *(bf16x8*)(hsel + c * D_DIM + w * 128 + q * 8 + ks * 32) = *(const bf16x8*)(src + ks * 32);
    }
  }
  asm volatile("s_waitcnt lgkmcnt(0)" ::: "memory");
  STAMP(3,3);
  f32x4 acc = {};
  {
    const char* base = wimg + w * 4096 + q * 256 + c * 8;
#pragma unroll
    for (int ks = 0; ks < 4; ++ks) {
      s16x4 lo = __builtin_bit_cast(s16x4, __builtin_amdgcn_ds_read_tr16_b64_v4i16(
                     (__attribute__((address_space(3))) s16x4*)(base + ks * 1024)));
      s16x4 hi = __builtin_bit_cast(s16x4, __builtin_amdgcn_ds_read_tr16_b64_v4i16(
                     (__attribute__((address_space(3))) s16x4*)(base + ks * 1024 + 128)));
      bf16x8 a = {lo[0], lo[1], lo[2], lo[3], hi[0], hi[1], hi[2], hi[3]};
      acc = __builtin_amdgcn_mfma_f32_16x16x32_bf16(a, xf[ks], acc, 0, 0, 0);
    }
  }
  *(f32x4*)(red + w * 1024 + l * 16) = acc;
  LDS_BARRIER();
  if (w == 0 && l < 32 && c < 8) {
    f32x4 g = *(const f32x4*)(red + l * 16);
    f32x4 u = *(const f32x4*)(red + (l + 32) * 16);
#pragma unroll
    for (int ww = 1; ww < 4; ++ww) {
      g += *(const f32x4*)(red + ww * 1024 + l * 16);
      u += *(const f32x4*)(red + ww * 1024 + (l + 32) * 16);
    }
    float o[4];
#pragma unroll
    for (int r = 0; r < 4; ++r) {
      const float gp = rbf(g[r]);
      const float sg = rbf(gp / (1.0f + __expf(-gp)));
      o[r] = sg * rbf(u[r]);
    }
    u32x2 ov = {pack_bf16(o[0], o[1]), pack_bf16(o[2], o[3])};
    *(u32x2*)(gu + c * F_DIM + f0 + q * 4) = ov;
  }
  STAMP(3,4);
}

__global__ __launch_bounds__(512) void k5_down(const u16* __restrict__ gu, const u16* wd_t, const u16* __restrict__ hnsel,
                                               const u16* __restrict__ hsel, const u32* __restrict__ selinfo,
                                               u16* __restrict__ out) {
  __shared__ __attribute__((aligned(1024))) char smem[8 * 7168 + 8 * 1024];
  char* wimg = smem;
  char* red = smem + 8 * 7168;
  const int b = blockIdx.x;
  const int tile = (b & 7) * 4 + (b >> 3);
  const int d0 = tile * 16;
  const int tid = threadIdx.x, w = __builtin_amdgcn_readfirstlane(tid >> 6), l = tid & 63;
  const int c = l & 15, q = l >> 4;
  STAMP(4,0);
  bf16x8 gf[7];
  {
    const u16* src = gu + (size_t)(c & 7) * F_DIM + w * 224 + q * 8;
#pragma unroll
    for (int ks = 0; ks < 7; ++ks) gf[ks] = *(const bf16x8*)(src + ks * 32);
  }
  u32x2 hnv = {0, 0}, hv = {0, 0};
  u32 row = 0;
  float sg = 0.f;
  if (w == 0) {
    hnv = *(const u32x2*)(hnsel + (c & 7) * D_DIM + d0 + q * 4);
    hv = *(const u32x2*)(hsel + (c & 7) * D_DIM + d0 + q * 4);
    row = selinfo[c & 7];
    sg = __uint_as_float(selinfo[8 + (c & 7)]);
  }
  {
    const char* src = (const char*)wd_t + (size_t)tile * (F_DIM * 32) + w * 7168 + l * 16;
#pragma unroll
    for (int m = 0; m < 7; ++m)
      __builtin_amdgcn_global_load_lds(GLB_PTR(src + m * 1024), LDS_PTR(wimg + w * 7168 + m * 1024), 16, 0, 0);
  }
  asm volatile("s_waitcnt vmcnt(0)" ::: "memory");
  STAMP(4,1);
  f32x4 acc = {};
  {
    const char* base = wimg + w * 7168 + q * 256 + c * 8;
#pragma unroll
    for (int ks = 0; ks < 7; ++ks) {
      s16x4 lo = __builtin_bit_cast(s16x4, __builtin_amdgcn_ds_read_tr16_b64_v4i16(
                     (__attribute__((address_space(3))) s16x4*)(base + ks * 1024)));
      s16x4 hi = __builtin_bit_cast(s16x4, __builtin_amdgcn_ds_read_tr16_b64_v4i16(
                     (__attribute__((address_space(3))) s16x4*)(base + ks * 1024 + 128)));
      bf16x8 a = {lo[0], lo[1], lo[2], lo[3], hi[0], hi[1], hi[2], hi[3]};
      acc = __builtin_amdgcn_mfma_f32_16x16x32_bf16(a, gf[ks], acc, 0, 0, 0);
    }
  }
  *(f32x4*)(red + w * 1024 + l * 16) = acc;
  LDS_BARRIER();
  if (w == 0 && c < 8) {
    f32x4 y = *(const f32x4*)(red + l * 16);
#pragma unroll
    for (int ww = 1; ww < 8; ++ww) y += *(const f32x4*)(red + ww * 1024 + l * 16);
    float hn4[4] = {bf_lo(hnv[0]), bf_hi(hnv[0]), bf_lo(hnv[1]), bf_hi(hnv[1])};
    float h4[4] = {bf_lo(hv[0]), bf_hi(hv[0]), bf_lo(hv[1]), bf_hi(hv[1])};
    float o[4];
#pragma unroll
    for (int r = 0; r < 4; ++r) {
      const float yy = rbf(rbf(y[r]) * sg);
      o[r] = h4[r] + rbf(hn4[r] + yy);
    }
    u32x2 ov = {pack_bf16(o[0], o[1]), pack_bf16(o[2], o[3])};
    *(u32x2*)(out + (size_t)row * D_DIM + d0 + q * 4) = ov;
  }
  STAMP(4,2);
}

extern "C" void kernel_launch(void* const* d_in, const int* in_sizes, int n_in, void* d_out, int out_size, void* d_ws,
                              size_t ws_size, hipStream_t stream) {
  const u16* x = (const u16*)d_in[0];
  const u16* Wsr = (const u16*)d_in[1];
  const u16* Wsi = (const u16*)d_in[2];
  const u16* Dr = (const u16*)d_in[3];
  const u16* Di = (const u16*)d_in[4];
  const u16* wr = (const u16*)d_in[6];
  const u16* wg = (const u16*)d_in[7];
  const u16* wu = (const u16*)d_in[8];
  const u16* wd = (const u16*)d_in[9];
  u16* out = (u16*)d_out;
  char* ws = (char*)d_ws;
  u16* abt = (u16*)(ws);
  u16* PQ = (u16*)(ws + (4u << 20));
  u16* hnbuf = (u16*)(ws + (12u << 20));
  u16* hbuf = (u16*)(ws + (14u << 20));
  u16* wgu_t = (u16*)(ws + (16u << 20));
  u16* wd_t = (u16*)(ws + (20u << 20));
  char* sm = ws + (22u << 20);
  u32* rkeys = (u32*)(sm);
  u16* gu = (u16*)(sm + 65536);
  u16* hnsel = (u16*)(sm + 131072);
  u16* hsel = (u16*)(sm + 196608);
  u32* selinfo = (u32*)(sm + 262144);

  k1_ln_gemm1<<<256, 512, 0, stream>>>(x, Dr, Di, abt, Wsr, Wsi);
  k2_gemm2<<<256, 512, 0, stream>>>(Wsr, Wsi, abt, PQ, wg, wu, wd, wgu_t, wd_t);
  k3_ln2_router<<<256, 512, 0, stream>>>(x, PQ, wr, out, hnbuf, hbuf, rkeys);
  k4_topk_up<<<224, 256, 0, stream>>>(rkeys, hnbuf, wgu_t, hbuf, gu, hnsel, hsel, selinfo);
  k5_down<<<32, 512, 0, stream>>>(gu, wd_t, hnsel, hsel, selinfo, out);
}
```

```cpp
#include <hip/hip_runtime.h>
#include <hip/hip_bf16.h>
#include <stdint.h>

typedef unsigned short u16;
typedef unsigned int u32;
typedef __attribute__((ext_vector_type(8))) short bf16x8;
typedef __attribute__((ext_vector_type(4))) short s16x4;
typedef __attribute__((ext_vector_type(4))) float f32x4;
typedef __attribute__((ext_vector_type(2))) float f32x2;
typedef __attribute__((ext_vector_type(2))) __bf16 bf16x2_t;
typedef __attribute__((ext_vector_type(4))) u32 u32x4;
typedef __attribute__((ext_vector_type(2))) u32 u32x2;

#define STAMP(K,I)

#define S_LEN 2048
#define D_DIM 512
#define F_DIM 1792

#define LDS_PTR(p) ((__attribute__((address_space(3))) void*)(p))
#define GLB_PTR(p) ((const __attribute__((address_space(1))) void*)(p))
#define SB() __builtin_amdgcn_sched_barrier(0)
#define CBAR() asm volatile("" ::: "memory")
#define IL1() __builtin_amdgcn_sched_group_barrier(0x008, 1, 0); __builtin_amdgcn_sched_group_barrier(0x100, 1, 0);
#define IL() do { IL1() IL1() IL1() IL1() IL1() IL1() IL1() IL1() __builtin_amdgcn_sched_group_barrier(0x008, 8, 0); } while (0)
#define LDS_BARRIER() asm volatile("s_waitcnt lgkmcnt(0)\n\ts_barrier" ::: "memory")

__device__ __forceinline__ u32 pack_bf16(float lo, float hi) {
  f32x2 v = {lo, hi};
  bf16x2_t b = __builtin_convertvector(v, bf16x2_t);
  return __builtin_bit_cast(u32, b);
}
__device__ __forceinline__ float bf_lo(u32 u) { return __uint_as_float(u << 16); }
__device__ __forceinline__ float bf_hi(u32 u) { return __uint_as_float(u & 0xffff0000u); }
__device__ __forceinline__ float rbf(float x) { return bf_lo(pack_bf16(x, 0.f)); }
__device__ __forceinline__ float dot2bf(u32 a, u32 b, float acc) {
  return __builtin_amdgcn_fdot2_f32_bf16(__builtin_bit_cast(bf16x2_t, a), __builtin_bit_cast(bf16x2_t, b), acc, false);
}

template <int CTRL>
__device__ __forceinline__ float dppf(float x) {
  return __builtin_bit_cast(float, __builtin_amdgcn_mov_dpp(__builtin_bit_cast(int, x), CTRL, 0xf, 0xf, true));
}
template <int CTRL>
__device__ __forceinline__ u32 dppu(u32 x) {
  return (u32)__builtin_amdgcn_mov_dpp((int)x, CTRL, 0xf, 0xf, true);
}
__device__ __forceinline__ float xrow_sum(float x) {
  auto s = __builtin_amdgcn_permlane16_swap(__float_as_uint(x), __float_as_uint(x), false, false);
  x = __uint_as_float(s[0]) + __uint_as_float(s[1]);
  auto t = __builtin_amdgcn_permlane32_swap(__float_as_uint(x), __float_as_uint(x), false, false);
  return __uint_as_float(t[0]) + __uint_as_float(t[1]);
}
__device__ __forceinline__ float wave_sum(float x) {
  x += dppf<0xB1>(x);
  x += dppf<0x4E>(x);
  x += dppf<0x141>(x);
  x += dppf<0x140>(x);
  return xrow_sum(x);
}
__device__ __forceinline__ u32 umax(u32 a, u32 b) { return a > b ? a : b; }
__device__ __forceinline__ u32 umin(u32 a, u32 b) { return a < b ? a : b; }
__device__ __forceinline__ u32 wave_max_u32(u32 x) {
  x = umax(x, dppu<0xB1>(x));
  x = umax(x, dppu<0x4E>(x));
  x = umax(x, dppu<0x141>(x));
  x = umax(x, dppu<0x140>(x));
  auto s = __builtin_amdgcn_permlane16_swap(x, x, false, false);
  x = umax(s[0], s[1]);
  auto t = __builtin_amdgcn_permlane32_swap(x, x, false, false);
  x = umax(t[0], t[1]);
  return x;
}
__device__ __forceinline__ void ln_scalars(float s1, float s2, float& mb, float& rinv) {
  const float mean = s1 * (1.0f / D_DIM);
  const float var = fmaxf(s2 * (1.0f / D_DIM) - mean * mean, 0.f);
  mb = rbf(mean);
  const float vb = rbf(var);
  const float sd = rbf(__builtin_amdgcn_sqrtf(rbf(vb + 1e-5f)));
  rinv = __builtin_amdgcn_rcpf(sd);
}

template <int N> __device__ __forceinline__ void wait_vm() { asm volatile("s_waitcnt vmcnt(%0)" ::"n"(N) : "memory"); }
template <int N> __device__ __forceinline__ void wait_vm_bar() { asm volatile("s_waitcnt vmcnt(%0) lgkmcnt(0)\n\ts_barrier" ::"n"(N) : "memory"); }

__global__ __launch_bounds__(512, 2) void k1_ln_gemm1(const u16* x, const u16* Dr, const u16* Di,
                                                      u16* __restrict__ abt, const u16* Wsr, const u16* Wsi) {
  __shared__ __attribute__((aligned(1024))) char smem[163840];
  char* wbuf = smem + 65536;
  const int b = blockIdx.x, xcd = b & 7, slot = b >> 3;
  const int Qd = xcd & 3, part = Qd >> 1, sh = Qd & 1;
  const int nt = (xcd >> 2) * 2 + (slot >> 4), ks0 = slot & 15;
  const int s0 = sh * 1024 + ks0 * 64;
  const int tid = threadIdx.x, w = __builtin_amdgcn_readfirstlane(tid >> 6), l = tid & 63;
  const int c = l & 15, q = l >> 4;
  const u16* Wp = part ? Di : Dr;
  STAMP(0,0);
  u32 pf0 = 0, pf1;
  if (w == 0) {
    const char* p = (const char*)(Wp + (size_t)(xcd >> 2) * 256 * D_DIM) + (size_t)slot * 8192 + l * 128;
    asm volatile("global_load_dword %0, %1, off" : "=v"(pf0) : "v"(p) : "memory");
  }
  {
    const char* xrow = (const char*)(x + (size_t)(s0 + w * 8) * D_DIM) + l * 16;
#pragma unroll
    for (int i = 0; i < 8; ++i)
      __builtin_amdgcn_global_load_lds(GLB_PTR(xrow + i * 1024), LDS_PTR(smem + (w * 8 + i) * 1024), 16, 0, 0);
  }
  const char* wsrc;
  {
    const int row = w * 16 + q;
    wsrc = (const char*)(Wp + (size_t)(nt * 128 + row) * D_DIM);
  }
  auto issueW = [&](int kq, int buf) {
#pragma unroll
    for (int jj = 0; jj < 4; ++jj) {
      const int rl = (q + 4 * jj) & 15;
      __builtin_amdgcn_global_load_lds(GLB_PTR(wsrc + jj * 4096 + kq * 256 + ((c ^ rl) * 16)),
                                       LDS_PTR(wbuf + buf * 32768 + (w * 4 + jj) * 1024), 16, 0, 0);
    }
  };
  issueW(0, 0);
  issueW(1, 1);
  issueW(2, 2);
  {
    const u16* Wsx = part ? Wsi : Wsr;
    const char* p = (const char*)(Wsx + (size_t)((xcd >> 2) * 1024 + slot * 32 + (tid >> 4)) * S_LEN + sh * 1024) + (tid & 15) * 128;
    asm volatile("global_load_dword %0, %1, off nt" : "=v"(pf1) : "v"(p) : "memory");
  }
  STAMP(0,1);
  wait_vm<13>();
  u32x4 xv[8];
#pragma unroll
  for (int i = 0; i < 8; ++i) xv[i] = *(const u32x4*)(smem + (w * 8 + i) * 1024 + l * 16);
  float s1[8], s2[8];
#pragma unroll
  for (int i = 0; i < 8; ++i) {
    float a = 0.f, bb = 0.f;
#pragma unroll
    for (int j = 0; j < 4; ++j) { a = dot2bf(xv[i][j], 0x3f803f80u, a); bb = dot2bf(xv[i][j], xv[i][j], bb); }
    s1[i] = a; s2[i] = bb;
  }
  {
    const bool o1 = l & 1, o2 = l & 2;
    float r1[4], r2[4];
#pragma unroll
    for (int a = 0; a < 4; ++a) {
      float t = o1 ? s1[2 * a + 1] : s1[2 * a], o = o1 ? s1[2 * a] : s1[2 * a + 1];
      r1[a] = t + dppf<0xB1>(o);
      t = o1 ? s2[2 * a + 1] : s2[2 * a]; o = o1 ? s2[2 * a] : s2[2 * a + 1];
      r2[a] = t + dppf<0xB1>(o);
    }
    float u1[2], u2[2];
#pragma unroll
    for (int m = 0; m < 2; ++m) {
      float t = o2 ? r1[2 * m + 1] : r1[2 * m], o = o2 ? r1[2 * m] : r1[2 * m + 1];
      u1[m] = t + dppf<0x4E>(o);
      t = o2 ? r2[2 * m + 1] : r2[2 * m]; o = o2 ? r2[2 * m] : r2[2 * m + 1];
      u2[m] = t + dppf<0x4E>(o);
    }
#pragma unroll
    for (int m = 0; m < 2; ++m) {
      u1[m] += dppf<0x124>(u1[m]); u2[m] += dppf<0x124>(u2[m]);
      u1[m] += dppf<0x128>(u1[m]); u2[m] += dppf<0x128>(u2[m]);
      u1[m] = xrow_sum(u1[m]); u2[m] = xrow_sum(u2[m]);
    }
    float mbv[2], riv[2];
#pragma unroll
    for (int m = 0; m < 2; ++m) ln_scalars(u1[m], u2[m], mbv[m], riv[m]);
#pragma unroll
    for (int i = 0; i < 8; ++i) {
      float mb, ri;
      switch (i & 3) {
        case 0: mb = dppf<0x00>(mbv[i >> 2]); ri = dppf<0x00>(riv[i >> 2]); break;
        case 1: mb = dppf<0x55>(mbv[i >> 2]); ri = dppf<0x55>(riv[i >> 2]); break;
        case 2: mb = dppf<0xAA>(mbv[i >> 2]); ri = dppf<0xAA>(riv[i >> 2]); break;
        default: mb = dppf<0xFF>(mbv[i >> 2]); ri = dppf<0xFF>(riv[i >> 2]); break;
      }
      u32x4 o;
#pragma unroll
      for (int j = 0; j < 4; ++j) {
        u32 dd = pack_bf16(bf_lo(xv[i][j]) - mb, bf_hi(xv[i][j]) - mb);
        o[j] = pack_bf16(bf_lo(dd) * ri, bf_hi(dd) * ri);
      }
      const int srow = w * 8 + i;
      const int g = 4 * ((srow >> 3) & 3) + (srow & 3);
      *(u32x4*)(smem + srow * 1024 + ((l ^ g) * 16)) = o;
    }
  }
  STAMP(0,2);
  wait_vm_bar<9>();
  STAMP(0,3);

  const int wsn = w & 1, wen = w >> 1;
  f32x4 acc[2][2] = {};
  {
    const char* abase = smem + (wsn * 32 + 8 * (c >> 2) + (c & 3)) * 1024;
    const char* bbase = wbuf + (wen * 32 + c) * 256;
    const int c0 = q ^ c;
    bf16x8 af[2][2], bfr[2][2];
    auto rd = [&](int st, int kq, int buf, int kl) {
      const int ch = ((4 * kl) ^ c0) * 16;
#pragma unroll
      for (int i = 0; i < 2; ++i) {
        af[st][i] = *(const bf16x8*)(abase + i * 4096 + kq * 256 + ch);
        bfr[st][i] = *(const bf16x8*)(bbase + buf * 32768 + i * 4096 + ch);
      }
    };
    auto mm = [&](int st) {
#pragma unroll
      for (int i = 0; i < 2; ++i)
#pragma unroll
        for (int j = 0; j < 2; ++j)
          acc[i][j] = __builtin_amdgcn_mfma_f32_16x16x32_bf16(af[st][i], bfr[st][j], acc[i][j], 0, 0, 0);
    };
    auto quarter = [&](int kq, int buf) {
      rd(0, kq, buf, 0);
      SB();
      rd(1, kq, buf, 1); mm(0); IL1() IL1() IL1() IL1() SB();
      rd(0, kq, buf, 2); mm(1); IL1() IL1() IL1() IL1() SB();
      rd(1, kq, buf, 3); mm(0); IL1() IL1() IL1() IL1() SB();
      mm(1);
      SB();
    };
    quarter(0, 0);
    wait_vm_bar<5>();
    issueW(3, 0);
    quarter(1, 1);
    wait_vm_bar<5>();
    quarter(2, 2);
    wait_vm_bar<0>();
    quarter(3, 0);
  }
  STAMP(0,4);
#pragma unroll
  for (int j = 0; j < 2; ++j) {
    const int e6 = (wen & 1) * 32 + j * 16 + c;
    const int ii = ((e6 >> 5) << 1) | ((e6 >> 2) & 1), qq = (e6 >> 3) & 3, rr = e6 & 3;
    const int p = (wen >> 1) * 64 + 16 * ii + 4 * qq + rr;
    char* dst = (char*)abt + (size_t)((Qd * 4 + nt) * 16 + ks0) * 16384 + p * 128;
    const int sw = (p >> 1) & 7;
    u32x4 o0;
    o0[0] = pack_bf16(acc[0][j][0], acc[0][j][1]); o0[1] = pack_bf16(acc[0][j][2], acc[0][j][3]);
    o0[2] = pack_bf16(acc[1][j][0], acc[1][j][1]); o0[3] = pack_bf16(acc[1][j][2], acc[1][j][3]);
    *(u32x4*)(dst + (((wsn * 4 + q) ^ sw) * 16)) = o0;
  }
  STAMP(0,5);
  asm volatile("" ::"v"(pf0), "v"(pf1));
  STAMP(0,6);
}

#define K2_STG 32768
#define K2_NST 4
template <int N> __device__ __forceinline__ void k2_waitbar() { asm volatile("s_waitcnt vmcnt(%0)\n\ts_barrier" ::"n"(N) : "memory"); }

__global__ __launch_bounds__(512, 2) void k2_gemm2(const u16* Wsr, const u16* Wsi, const u16* abt, u16* __restrict__ PQ,
                                                   const u16* wg, const u16* wu, const u16* wd,
                                                   u16* __restrict__ wd_t) {
  __shared__ __attribute__((aligned(1024))) char smem[K2_NST * K2_STG];
  const int b = blockIdx.x, xcd = b & 7, slot = b >> 3;
  const int Qd = xcd & 3, part = Qd >> 1, sh = Qd & 1;
  const int mt = slot >> 2, nt = slot & 3;
  const int t0 = ((xcd >> 2) * 8 + mt) * 128;
  const int tid = threadIdx.x, w = __builtin_amdgcn_readfirstlane(tid >> 6), l = tid & 63;
  const int c = l & 15, q = l >> 4;
  constexpr int D = K2_NST - 1;
  const int k0 = slot & 15;
  STAMP(1,0);
  if (w >= 4) {
    const int lw = w - 4;
    const char* Abase = (const char*)((part ? Wsi : Wsr) + (size_t)t0 * S_LEN + sh * 1024);
    const char* Bbase = (const char*)abt + (size_t)(Qd * 4 + nt) * 16 * 16384;
    const u32 av0 = (u32)(l >> 3) * 4096u + (u32)((l & 7) ^ (l >> 4)) * 16u;
    const u32 av1 = (u32)(l >> 3) * 4096u + (u32)((l & 7) ^ (4 + (l >> 4))) * 16u;
    const u32 bv = (u32)l * 16u;
    auto issue = [&](int k, int buf) {
      const int kstep = (k + k0) & 15;
      const char* ab = Abase + kstep * 128;
      const char* bb = Bbase + (size_t)kstep * 16384;
      char* lb = smem + buf * K2_STG;
#pragma unroll
      for (int jj = 0; jj < 4; ++jj) {
        const int ja = lw * 4 + jj;
        __builtin_amdgcn_global_load_lds(GLB_PTR(bb + ja * 1024 + bv), LDS_PTR(lb + 16384 + ja * 1024), 16, 0, 0);
        __builtin_amdgcn_global_load_lds(GLB_PTR(ab + (size_t)ja * 32768 + ((jj & 1) ? av1 : av0)), LDS_PTR(lb + ja * 1024), 16, 0, 0);
      }
    };
#pragma unroll 1
    for (int s = 0; s < D; ++s) issue(s, s);
    int nbuf = D;
#pragma unroll 1
    for (int k = 0; k < 16 - D; ++k) {
      k2_waitbar<(D - 1) * 8>();
      issue(k + D, nbuf);
      nbuf = (nbuf + 1 == K2_NST) ? 0 : nbuf + 1;
    }
    const int lt = tid - 256;
    u32x4 rv[2];
    int dsto[2];
#pragma unroll
    for (int i = 0; i < 2; ++i) {
      int idx = lt + i * 256;
      if (idx > 447) idx = 447;
      const int ch = idx & 7, r = slot * 56 + (idx >> 3);
      const u16* src = wd + (size_t)r * D_DIM + xcd * 64 + ch * 8;
      rv[i] = __builtin_nontemporal_load((const u32x4*)src);
      dsto[i] = ((xcd * 8 + ch) * F_DIM + r) * 8;
    }
    u32 pf;
    {
      const int i2 = lt & 127;
      const int r = slot * 16 + (i2 >> 3), mat = (i2 >> 2) & 1, ln = i2 & 3;
      const char* p = (const char*)((mat ? wu : wg) + (size_t)r * F_DIM) + ((xcd * 448) & ~127) + ln * 128;
      pf = __builtin_nontemporal_load((const u32*)p);
    }
    CBAR();
    k2_waitbar<2 * 8 + 3>();
    k2_waitbar<1 * 8 + 3>();
    k2_waitbar<3>();
#pragma unroll
    for (int i = 0; i < 2; ++i) *(u32x4*)(wd_t + dsto[i]) = rv[i];
    asm volatile("" ::"v"(pf));
    STAMP(1,1); STAMP(1,2); STAMP(1,3);
    return;
  }
  const int we = w & 1, wt = w >> 1;
  f32x4 acc[4][4] = {};
  const int sw = c >> 1;
  const int ro0 = c * 128 + ((q ^ sw) * 16);
  const int ro1 = c * 128 + (((q + 4) ^ sw) * 16);
  const int aoff = 16384 + we * 8192, boff = wt * 8192;
  bf16x8 a0[4], b0[4], a1[4], b1[4];
  auto rd = [&](bf16x8* af, bf16x8* bf, int buf, int ro) {
    const char* lb = smem + buf * K2_STG + ro;
#pragma unroll
    for (int i = 0; i < 4; ++i) { af[i] = *(const bf16x8*)(lb + aoff + i * 2048); bf[i] = *(const bf16x8*)(lb + boff + i * 2048); }
  };
  auto mm = [&](const bf16x8* af, const bf16x8* bf) {
#pragma unroll
    for (int i = 0; i < 4; ++i)
#pragma unroll
      for (int j = 0; j < 4; ++j)
        acc[i][j] = __builtin_amdgcn_mfma_f32_16x16x32_bf16(af[i], bf[j], acc[i][j], 0, 0, 0);
  };
  asm volatile("s_barrier" ::: "memory");
  STAMP(1,1);
  {
    int buf = 0;
    rd(a0, b0, 0, ro0);
#pragma unroll 1
    for (int k = 0; k < 15; ++k) {
      rd(a1, b1, buf, ro1);
      mm(a0, b0);
      IL();
      SB();
      LDS_BARRIER();
      buf = (buf + 1 == K2_NST) ? 0 : buf + 1;
      rd(a0, b0, buf, ro0);
      mm(a1, b1);
      IL();
      SB();
    }
    rd(a1, b1, buf, ro1);
    mm(a0, b0);
    IL();
    SB();
    mm(a1, b1);
  }
  STAMP(1,2);
#pragma unroll
  for (int j = 0; j < 4; ++j) {
    const int t = t0 + wt * 64 + j * 16 + c;
    u16* dst = PQ + (size_t)Qd * (S_LEN * D_DIM) + (size_t)t * D_DIM + nt * 128 + we * 64 + q * 8;
    u32x4 o0, o1;
    o0[0] = pack_bf16(acc[0][j][0], acc[0][j][1]); o0[1] = pack_bf16(acc[0][j][2], acc[0][j][3]);
    o0[2] = pack_bf16(acc[1][j][0], acc[1][j][1]); o0[3] = pack_bf16(acc[1][j][2], acc[1][j][3]);
    o1[0] = pack_bf16(acc[2][j][0], acc[2][j][1]); o1[1] = pack_bf16(acc[2][j][2], acc[2][j][3]);
    o1[2] = pack_bf16(acc[3][j][0], acc[3][j][1]); o1[3] = pack_bf16(acc[3][j][2], acc[3][j][3]);
    *(u32x4*)(dst) = o0;
    *(u32x4*)(dst + 32) = o1;
  }
  STAMP(1,3);
}

__device__ __forceinline__ u32x4 compute_h8(const u32x4& p0, const u32x4& p1, const u32x4& q0, const u32x4& q1, const u32x4& xv) {
  u32x4 h;
#pragma unroll
  for (int j = 0; j < 4; ++j) {
    u32 p = pack_bf16(bf_lo(p0[j]) + bf_lo(p1[j]), bf_hi(p0[j]) + bf_hi(p1[j]));
    u32 qq = pack_bf16(bf_lo(q0[j]) + bf_lo(q1[j]), bf_hi(q0[j]) + bf_hi(q1[j]));
    u32 y = pack_bf16(bf_lo(p) - bf_lo(qq), bf_hi(p) - bf_hi(qq));
    h[j] = pack_bf16(bf_lo(xv[j]) + bf_lo(y), bf_hi(xv[j]) + bf_hi(y));
  }
  return h;
}

__global__ __launch_bounds__(512) void k3_ln2_router(const u16* __restrict__ x, const u16* __restrict__ PQ,
                                                     const u16* __restrict__ wr, u16* __restrict__ out,
                                                     u16* __restrict__ hnbuf, u32* __restrict__ rkeys) {
  const int tid = threadIdx.x, w = tid >> 6, l = tid & 63;
  const int b = blockIdx.x, xcd = b & 7, slot = b >> 3;
  const int row = xcd * 256 + slot * 8 + w;
  STAMP(2,0);
  const size_t off = (size_t)row * D_DIM + l * 8;
  const size_t PS = (size_t)S_LEN * D_DIM;
  const u32x4 p0 = *(const u32x4*)(PQ + off);
  const u32x4 p1 = *(const u32x4*)(PQ + PS + off);
  const u32x4 q0 = *(const u32x4*)(PQ + 2 * PS + off);
  const u32x4 q1 = *(const u32x4*)(PQ + 3 * PS + off);
  const u32x4 xv = *(const u32x4*)(x + off);
  const u32x4 wv = *(const u32x4*)(wr + l * 8);
  const u32x4 h = compute_h8(p0, p1, q0, q1, xv);
  STAMP(2,1);
  float s1 = 0.f, s2 = 0.f;
#pragma unroll
  for (int j = 0; j < 4; ++j) { s1 = dot2bf(h[j], 0x3f803f80u, s1); s2 = dot2bf(h[j], h[j], s2); }
  s1 = wave_sum(s1);
  s2 = wave_sum(s2);
  float mb, rinv;
  ln_scalars(s1, s2, mb, rinv);
  u32x4 hn, o;
  float dot = 0.f;
#pragma unroll
  for (int j = 0; j < 4; ++j) {
    u32 dd = pack_bf16(bf_lo(h[j]) - mb, bf_hi(h[j]) - mb);
    u32 n = pack_bf16(bf_lo(dd) * rinv, bf_hi(dd) * rinv);
    hn[j] = n;
    dot = dot2bf(n, wv[j], dot);
    o[j] = pack_bf16(bf_lo(h[j]) + bf_lo(n), bf_hi(h[j]) + bf_hi(n));
  }
  *(u32x4*)(out + off) = o;
  *(u32x4*)(hnbuf + off) = hn;
  dot = wave_sum(dot);
  if (l == 0) {
    u32 u = pack_bf16(dot, 0.f) & 0xffffu;
    u32 key = (u & 0x8000u) ? (~u & 0xffffu) : (u | 0x8000u);
    rkeys[row] = (key << 16) | (u32)(S_LEN - 1 - row);
  }
  STAMP(2,2);
}

#define CE(a, b) { u32 _hi = umax(a, b), _lo = umin(a, b); a = _hi; b = _lo; }

__global__ __launch_bounds__(256) void k4_topk_up(const u32* __restrict__ rkeys, const u16* __restrict__ hnbuf,
                                                  const u16* wg, const u16* wu, const u16* __restrict__ x,
                                                  const u16* __restrict__ PQ, u16* __restrict__ gu,
                                                  u16* __restrict__ hnsel, u16* __restrict__ hsel,
                                                  u32* __restrict__ selinfo) {
  __shared__ __attribute__((aligned(1024))) char smem[4 * 4096 + 4 * 1024 + 512];
  char* wimg = smem;
  char* red = smem + 16384;
  u32* cand = (u32*)(smem + 16384 + 4096);
  u32* gmax = cand + 64;
  u32* cnt = cand + 72;
  u32* sel = cand + 80;
  const int b = blockIdx.x;
  const int ct = (b & 7) * 28 + (b >> 3);
  const int f0 = ct * 8;
  const int tid = threadIdx.x, w = __builtin_amdgcn_readfirstlane(tid >> 6), l = tid & 63;
  const int c = l & 15, q = l >> 4;
  STAMP(3,0);
  const u32x4 k0 = *(const u32x4*)(rkeys + tid * 8);
  const u32x4 k1 = *(const u32x4*)(rkeys + tid * 8 + 4);
  CBAR();
  u32x4 wreg[4];
  {
    const u16* src = ((l & 1) ? wu : wg) + (size_t)(w * 128 + (l >> 1)) * F_DIM + f0;
#pragma unroll
    for (int m = 0; m < 4; ++m) wreg[m] = *(const u32x4*)(src + (size_t)m * 32 * F_DIM);
  }
  CBAR();
  if (tid < 64) cand[tid] = 0;
  if (tid == 0) *cnt = 0;
  u32 a0 = k0[0], a1 = k0[1], a2 = k0[2], a3 = k0[3], a4 = k1[0], a5 = k1[1], a6 = k1[2], a7 = k1[3];
  STAMP(3,1);
  {
    u32 m = umax(umax(umax(a0, a1), umax(a2, a3)), umax(umax(a4, a5), umax(a6, a7)));
    m = umax(m, dppu<0xB1>(m));
    m = umax(m, dppu<0x4E>(m));
    m = umax(m, dppu<0x141>(m));
    m = umax(m, dppu<0x140>(m));
    auto sx = __builtin_amdgcn_permlane16_swap(m, m, false, false);
    m = umax(sx[0], sx[1]);
    if ((l & 31) == 0) gmax[w * 2 + (l >> 5)] = m;
  }
  LDS_BARRIER();
  {
    const u32x4 g0 = *(const u32x4*)(gmax), g1 = *(const u32x4*)(gmax + 4);
    const u32 T = umin(umin(umin(g0[0], g0[1]), umin(g0[2], g0[3])), umin(umin(g1[0], g1[1]), umin(g1[2], g1[3])));
    const u32 av[8] = {a0, a1, a2, a3, a4, a5, a6, a7};
    int n = 0;
#pragma unroll
    for (int j = 0; j < 8; ++j) n += (av[j] >= T);
    if (n > 0) {
      u32 pos = __hip_atomic_fetch_add(cnt, (u32)n, __ATOMIC_RELAXED, __HIP_MEMORY_SCOPE_WORKGROUP);
#pragma unroll
      for (int j = 0; j < 8; ++j) {
        if (av[j] >= T) { if (pos < 64) cand[pos] = av[j]; ++pos; }
      }
    }
  }
  LDS_BARRIER();
  u32 selkey;
  {
    const u32 n = __builtin_amdgcn_readfirstlane(*cnt);
    if (n <= 64) {
      const u32 mine = cand[l];
      int rank = 0;
      for (u32 i0 = 0; i0 < n; i0 += 16) {
#pragma unroll
        for (int i = 0; i < 4; ++i) {
          const u32x4 v = *(const u32x4*)(cand + i0 + i * 4);
          rank += (v[0] > mine) + (v[1] > mine) + (v[2] > mine) + (v[3] > mine);
        }
      }
      if (rank < 8 && (u32)l < n) sel[w * 8 + rank] = mine;
      asm volatile("s_waitcnt lgkmcnt(0)" ::: "memory");
      selkey = sel[w * 8 + (l & 7)];
    } else {
      CE(a0, a1) CE(a2, a3) CE(a4, a5) CE(a6, a7)
      CE(a0, a2) CE(a1, a3) CE(a4, a6) CE(a5, a7)
      CE(a1, a2) CE(a5, a6) CE(a0, a4) CE(a3, a7)
      CE(a1, a5) CE(a2, a6)
      CE(a1, a4) CE(a3, a6)
      CE(a2, a4) CE(a3, a5)
      CE(a3, a4)
      u32 mycand = 0;
#pragma unroll 1
      for (int it = 0; it < 8; ++it) {
        const u32 m = wave_max_u32(a0);
        const bool win = (a0 == m);
        a0 = win ? a1 : a0; a1 = win ? a2 : a1; a2 = win ? a3 : a2; a3 = win ? a4 : a3;
        a4 = win ? a5 : a4; a5 = win ? a6 : a5; a6 = win ? a7 : a6; a7 = win ? 0u : a7;
        mycand = (l == it) ? m : mycand;
      }
      LDS_BARRIER();
      if (l < 8) cand[w * 8 + l] = mycand;
      LDS_BARRIER();
      const u32 mine = cand[l & 31];
      int rank = 0;
#pragma unroll 1
      for (int i = 0; i < 8; ++i) {
        const u32x4 v = *(const u32x4*)(cand + i * 4);
        rank += (v[0] > mine) + (v[1] > mine) + (v[2] > mine) + (v[3] > mine);
      }
      if (rank < 8 && l < 32) sel[w * 8 + rank] = mine;
      asm volatile("s_waitcnt lgkmcnt(0)" ::: "memory");
      selkey = sel[w * 8 + (l & 7)];
    }
  }
  STAMP(3,2);
  const int myrow = S_LEN - 1 - (int)(selkey & 0xffffu);
  bf16x8 xf[4];
  {
    const u16* src = hnbuf + (size_t)myrow * D_DIM + w * 128 + q * 8;
#pragma unroll
    for (int ks = 0; ks < 4; ++ks) xf[ks] = *(const bf16x8*)(src + ks * 32);
  }
  CBAR();
#pragma unroll
  for (int m = 0; m < 4; ++m) *(u32x4*)(wimg + w * 4096 + m * 1024 + l * 16) = wreg[m];
  if (b == 0) {
    if (c < 8) {
#pragma unroll
      for (int ks = 0; ks < 4; ++ks)
        *(bf16x8*)(hnsel + c * D_DIM + w * 128 + q * 8 + ks * 32) = xf[ks];
    }
    if (tid < 8) {
      const u32 key = selkey >> 16;
      const u32 u = (key & 0x8000u) ? (key & 0x7fffu) : (~key & 0xffffu);
      const float r = __uint_as_float(u << 16);
      const float sg = rbf(1.0f / (1.0f + __expf(-r)));
      selinfo[tid] = (u32)myrow;
      selinfo[8 + tid] = __float_as_uint(sg);
    }
  }
  if (b == 1) {
    const size_t PS = (size_t)S_LEN * D_DIM;
#pragma unroll
    for (int i = 0; i < 2; ++i) {
      const int tok = w * 2 + i;
      const int row = __builtin_amdgcn_readlane(myrow, tok);
      const size_t off = (size_t)row * D_DIM + l * 8;
      const u32x4 h = compute_h8(*(const u32x4*)(PQ + off), *(const u32x4*)(PQ + PS + off), *(const u32x4*)(PQ + 2 * PS + off),
                                 *(const u32x4*)(PQ + 3 * PS + off), *(const u32x4*)(x + off));
      *(u32x4*)(hsel + tok * D_DIM + l * 8) = h;
    }
  }
  asm volatile("s_waitcnt lgkmcnt(0)" ::: "memory");
  STAMP(3,3);
  f32x4 acc = {};
  {
    const char* base = wimg + w * 4096 + q * 256 + c * 8;
#pragma unroll
    for (int ks = 0; ks < 4; ++ks) {
      s16x4 lo = __builtin_bit_cast(s16x4, __builtin_amdgcn_ds_read_tr16_b64_v4i16(
                     (__attribute__((address_space(3))) s16x4*)(base + ks * 1024)));
      s16x4 hi = __builtin_bit_cast(s16x4, __builtin_amdgcn_ds_read_tr16_b64_v4i16(
                     (__attribute__((address_space(3))) s16x4*)(base + ks * 1024 + 128)));
      bf16x8 a = {lo[0], lo[1], lo[2], lo[3], hi[0], hi[1], hi[2], hi[3]};
      acc = __builtin_amdgcn_mfma_f32_16x16x32_bf16(a, xf[ks], acc, 0, 0, 0);
    }
  }
  *(f32x4*)(red + w * 1024 + l * 16) = acc;
  LDS_BARRIER();
  if (w == 0 && l < 32 && c < 8) {
    f32x4 g = *(const f32x4*)(red + l * 16);
    f32x4 u = *(const f32x4*)(red + (l + 32) * 16);
#pragma unroll
    for (int ww = 1; ww < 4; ++ww) {
      g += *(const f32x4*)(red + ww * 1024 + l * 16);
      u += *(const f32x4*)(red + ww * 1024 + (l + 32) * 16);
    }
    float o[4];
#pragma unroll
    for (int r = 0; r < 4; ++r) {
      const float gp = rbf(g[r]);
      const float sg = rbf(gp / (1.0f + __expf(-gp)));
      o[r] = sg * rbf(u[r]);
    }
    const int kk = (f0 + q * 4) >> 1;
    u16* dst = gu + ((kk >> 5) * 64 + ((kk >> 3) & 3) * 16 + c) * 8 + (kk & 7);
    *(u32*)(dst) = pack_bf16(o[0], o[2]);
    *(u32*)(dst + 64) = pack_bf16(o[1], o[3]);
  }
  STAMP(3,4);
}

__global__ __launch_bounds__(256) void k5_down(const u16* __restrict__ gu, const u16* wd_t, const u16* __restrict__ hnsel,
                                               const u16* __restrict__ hsel, const u32* __restrict__ selinfo,
                                               u16* __restrict__ out) {
  __shared__ __attribute__((aligned(1024))) char smem[4 * 7168 + 4 * 1024];
  char* wimg = smem;
  char* red = smem + 4 * 7168;
  const int b = blockIdx.x;
  const int tile = (b & 7) * 8 + (b >> 3);
  const int d0 = tile * 8;
  const int tid = threadIdx.x, w = __builtin_amdgcn_readfirstlane(tid >> 6), l = tid & 63;
  const int c = l & 15, q = l >> 4;
  STAMP(4,0);
  bf16x8 gf[7];
  {
    const u16* src = gu + (size_t)(w * 7 * 64 + l) * 8;
#pragma unroll
    for (int ks = 0; ks < 7; ++ks) gf[ks] = *(const bf16x8*)(src + ks * 512);
  }
  u32x2 hnv = {0, 0}, hv = {0, 0};
  u32 row = 0;
  float sg = 0.f;
  if (w == 0) {
    hnv = *(const u32x2*)(hnsel + (c & 7) * D_DIM + d0 + (q & 1) * 4);
    hv = *(const u32x2*)(hsel + (c & 7) * D_DIM + d0 + (q & 1) * 4);
    row = selinfo[c & 7];
    sg = __uint_as_float(selinfo[8 + (c & 7)]);
  }
  {
    const char* src = (const char*)wd_t + (size_t)tile * (F_DIM * 16) + w * 7168 + l * 16;
#pragma unroll
    for (int m = 0; m < 7; ++m)
      __builtin_amdgcn_global_load_lds(GLB_PTR(src + m * 1024), LDS_PTR(wimg + w * 7168 + m * 1024), 16, 0, 0);
  }
  asm volatile("s_waitcnt vmcnt(0)" ::: "memory");
  STAMP(4,1);
  f32x4 acc = {};
  {
    const char* base = wimg + w * 7168 + q * 256 + c * 8;
#pragma unroll
    for (int ks = 0; ks < 7; ++ks) {
      s16x4 lo = __builtin_bit_cast(s16x4, __builtin_amdgcn_ds_read_tr16_b64_v4i16(
                     (__attribute__((address_space(3))) s16x4*)(base + ks * 1024)));
      s16x4 hi = __builtin_bit_cast(s16x4, __builtin_amdgcn_ds_read_tr16_b64_v4i16(
                     (__attribute__((address_space(3))) s16x4*)(base + ks * 1024 + 128)));
      bf16x8 a = {lo[0], lo[1], lo[2], lo[3], hi[0], hi[1], hi[2], hi[3]};
      acc = __builtin_amdgcn_mfma_f32_16x16x32_bf16(a, gf[ks], acc, 0, 0, 0);
    }
  }
  *(f32x4*)(red + w * 1024 + l * 16) = acc;
  LDS_BARRIER();
  if (w == 0 && c < 8 && q < 2) {
    f32x4 y = *(const f32x4*)(red + l * 16) + *(const f32x4*)(red + (l + 40) * 16);
#pragma unroll
    for (int ww = 1; ww < 4; ++ww)
      y += *(const f32x4*)(red + ww * 1024 + l * 16) + *(const f32x4*)(red + ww * 1024 + (l + 40) * 16);
    float hn4[4] = {bf_lo(hnv[0]), bf_hi(hnv[0]), bf_lo(hnv[1]), bf_hi(hnv[1])};
    float h4[4] = {bf_lo(hv[0]), bf_hi(hv[0]), bf_lo(hv[1]), bf_hi(hv[1])};
    float o[4];
#pragma unroll
    for (int r = 0; r < 4; ++r) {
      const float yy = rbf(rbf(y[r]) * sg);
      o[r] = h4[r] + rbf(hn4[r] + yy);
    }
    u32x2 ov = {pack_bf16(o[0], o[1]), pack_bf16(o[2], o[3])};
    *(u32x2*)(out + (size_t)row * D_DIM + d0 + q * 4) = ov;
  }
  STAMP(4,2);
}

extern "C" void kernel_launch(void* const* d_in, const int* in_sizes, int n_in, void* d_out, int out_size, void* d_ws,
                              size_t ws_size, hipStream_t stream) {
  const u16* x = (const u16*)d_in[0];
  const u16* Wsr = (const u16*)d_in[1];
  const u16* Wsi = (const u16*)d_in[2];
  const u16* Dr = (const u16*)d_in[3];
  const u16* Di = (const u16*)d_in[4];
  const u16* wr = (const u16*)d_in[6];
  const u16* wg = (const u16*)d_in[7];
  const u16* wu = (const u16*)d_in[8];
  const u16* wd = (const u16*)d_in[9];
  u16* out = (u16*)d_out;
  char* ws = (char*)d_ws;
  u16* abt = (u16*)(ws);
  u16* PQ = (u16*)(ws + (4u << 20));
  u16* hnbuf = (u16*)(ws + (12u << 20));
  u16* wd_t = (u16*)(ws + (14u << 20));
  char* sm = ws + (16u << 20);
  u32* rkeys = (u32*)(sm);
  u16* gu = (u16*)(sm + 65536);
  u16* hnsel = (u16*)(sm + 131072);
  u16* hsel = (u16*)(sm + 196608);
  u32* selinfo = (u32*)(sm + 262144);

  k1_ln_gemm1<<<256, 512, 0, stream>>>(x, Dr, Di, abt, Wsr, Wsi);
  k2_gemm2<<<256, 512, 0, stream>>>(Wsr, Wsi, abt, PQ, wg, wu, wd, wd_t);
  k3_ln2_router<<<256, 512, 0, stream>>>(x, PQ, wr, out, hnbuf, rkeys);
  k4_topk_up<<<224, 256, 0, stream>>>(rkeys, hnbuf, wg, wu, x, PQ, gu, hnsel, hsel, selinfo);
  k5_down<<<64, 256, 0, stream>>>(gu, wd_t, hnsel, hsel, selinfo, out);
}
```

```cpp
#include <hip/hip_runtime.h>
#include <hip/hip_bf16.h>
#include <stdint.h>

typedef unsigned short u16;
typedef unsigned int u32;
typedef __attribute__((ext_vector_type(8))) short bf16x8;
typedef __attribute__((ext_vector_type(4))) short s16x4;
typedef __attribute__((ext_vector_type(4))) float f32x4;
typedef __attribute__((ext_vector_type(2))) float f32x2;
typedef __attribute__((ext_vector_type(2))) __bf16 bf16x2_t;
typedef __attribute__((ext_vector_type(4))) u32 u32x4;
typedef __attribute__((ext_vector_type(2))) u32 u32x2;

#define STAMP(K,I)

#define S_LEN 2048
#define D_DIM 512
#define F_DIM 1792

#define LDS_PTR(p) ((__attribute__((address_space(3))) void*)(p))
#define GLB_PTR(p) ((const __attribute__((address_space(1))) void*)(p))
#define SB() __builtin_amdgcn_sched_barrier(0)
#define CBAR() asm volatile("" ::: "memory")
#define IL1() __builtin_amdgcn_sched_group_barrier(0x008, 1, 0); __builtin_amdgcn_sched_group_barrier(0x100, 1, 0);
#define IL() do { IL1() IL1() IL1() IL1() IL1() IL1() IL1() IL1() __builtin_amdgcn_sched_group_barrier(0x008, 8, 0); } while (0)
#define LDS_BARRIER() asm volatile("s_waitcnt lgkmcnt(0)\n\ts_barrier" ::: "memory")

__device__ __forceinline__ u32 pack_bf16(float lo, float hi) {
  f32x2 v = {lo, hi};
  bf16x2_t b = __builtin_convertvector(v, bf16x2_t);
  return __builtin_bit_cast(u32, b);
}
__device__ __forceinline__ float bf_lo(u32 u) { return __uint_as_float(u << 16); }
__device__ __forceinline__ float bf_hi(u32 u) { return __uint_as_float(u & 0xffff0000u); }
__device__ __forceinline__ float rbf(float x) { return bf_lo(pack_bf16(x, 0.f)); }
#define ST_POLICY 0
__device__ __forceinline__ void store16_wt(void* p, u32x4 v) {
  *(u32x4*)p = v;
}
__device__ __forceinline__ float dot2bf(u32 a, u32 b, float acc) {
  return __builtin_amdgcn_fdot2_f32_bf16(__builtin_bit_cast(bf16x2_t, a), __builtin_bit_cast(bf16x2_t, b), acc, false);
}

template <int CTRL>
__device__ __forceinline__ float dppf(float x) {
  return __builtin_bit_cast(float, __builtin_amdgcn_mov_dpp(__builtin_bit_cast(int, x), CTRL, 0xf, 0xf, true));
}
template <int CTRL>
__device__ __forceinline__ u32 dppu(u32 x) {
  return (u32)__builtin_amdgcn_mov_dpp((int)x, CTRL, 0xf, 0xf, true);
}
__device__ __forceinline__ float xrow_sum(float x) {
  auto s = __builtin_amdgcn_permlane16_swap(__float_as_uint(x), __float_as_uint(x), false, false);
  x = __uint_as_float(s[0]) + __uint_as_float(s[1]);
  auto t = __builtin_amdgcn_permlane32_swap(__float_as_uint(x), __float_as_uint(x), false, false);
  return __uint_as_float(t[0]) + __uint_as_float(t[1]);
}
__device__ __forceinline__ float wave_sum(float x) {
  x += dppf<0xB1>(x);
  x += dppf<0x4E>(x);
  x += dppf<0x141>(x);
  x += dppf<0x140>(x);
  return xrow_sum(x);
}
__device__ __forceinline__ u32 umax(u32 a, u32 b) { return a > b ? a : b; }
__device__ __forceinline__ u32 umin(u32 a, u32 b) { return a < b ? a : b; }
__device__ __forceinline__ u32 wave_max_u32(u32 x) {
  x = umax(x, dppu<0xB1>(x));
  x = umax(x, dppu<0x4E>(x));
  x = umax(x, dppu<0x141>(x));
  x = umax(x, dppu<0x140>(x));
  auto s = __builtin_amdgcn_permlane16_swap(x, x, false, false);
  x = umax(s[0], s[1]);
  auto t = __builtin_amdgcn_permlane32_swap(x, x, false, false);
  x = umax(t[0], t[1]);
  return x;
}
__device__ __forceinline__ void ln_scalars(float s1, float s2, float& mb, float& rinv) {
  const float mean = s1 * (1.0f / D_DIM);
  const float var = fmaxf(s2 * (1.0f / D_DIM) - mean * mean, 0.f);
  mb = rbf(mean);
  const float vb = rbf(var);
  const float sd = rbf(__builtin_amdgcn_sqrtf(rbf(vb + 1e-5f)));
  rinv = __builtin_amdgcn_rcpf(sd);
}

template <int N> __device__ __forceinline__ void wait_vm_bar() { asm volatile("s_waitcnt vmcnt(%0) lgkmcnt(0)\n\ts_barrier" ::"n"(N) : "memory"); }
#define VM_DRAIN() asm volatile("s_waitcnt vmcnt(0)" ::: "memory")

__device__ __forceinline__ void st16_dev(void* p, u32x4 v) {
  asm volatile("global_store_dwordx4 %0, %1, off sc1\n\ts_nop 1" ::"v"(p), "v"(v) : "memory");
}
__device__ __forceinline__ void st8_dev(void* p, u32x2 v) {
  asm volatile("global_store_dwordx2 %0, %1, off sc1\n\ts_nop 1" ::"v"(p), "v"(v) : "memory");
}
__device__ __forceinline__ void st4_dev(void* p, u32 v) {
  asm volatile("global_store_dword %0, %1, off sc1" ::"v"(p), "v"(v) : "memory");
}
typedef __attribute__((address_space(1))) const u32x4 gc_u32x4;
__device__ __forceinline__ u32 ld4_dev_wait(const void* p) {
  u32 v;
  asm volatile("global_load_dword %0, %1, off sc1\n\ts_waitcnt vmcnt(0)" : "=&v"(v) : "v"(p) : "memory");
  return v;
}
__device__ __forceinline__ u32x4 ld16_dev_wait(const void* p) {
  u32x4 v;
  asm volatile("global_load_dwordx4 %0, %1, off sc1\n\ts_waitcnt vmcnt(0)" : "=&v"(v) : "v"(p) : "memory");
  return v;
}
#define SPIN_CAP 20000
__device__ __forceinline__ void poll_words(const u32* p, u32 tag) {
#pragma unroll 1
  for (int spin = 0; spin < SPIN_CAP; ++spin) {
    const u32 v = ld4_dev_wait(p);
    if (__all(v == tag)) break;
    __builtin_amdgcn_s_sleep(1);
  }
}

__device__ __forceinline__ u32x4 compute_h8(const u32x4& p0, const u32x4& p1, const u32x4& q0, const u32x4& q1, const u32x4& xv) {
  u32x4 h;
#pragma unroll
  for (int j = 0; j < 4; ++j) {
    u32 p = pack_bf16(bf_lo(p0[j]) + bf_lo(p1[j]), bf_hi(p0[j]) + bf_hi(p1[j]));
    u32 qq = pack_bf16(bf_lo(q0[j]) + bf_lo(q1[j]), bf_hi(q0[j]) + bf_hi(q1[j]));
    u32 y = pack_bf16(bf_lo(p) - bf_lo(qq), bf_hi(p) - bf_hi(qq));
    h[j] = pack_bf16(bf_lo(xv[j]) + bf_lo(y), bf_hi(xv[j]) + bf_hi(y));
  }
  return h;
}
#define CE(a, b) { u32 _hi = umax(a, b), _lo = umin(a, b); a = _hi; b = _lo; }

#define WS_ABT 0
#define WS_PQ (4u << 20)
#define WS_HN (12u << 20)
#define WS_CTL (14u << 20)
#define CTL_EPOCH 0
#define CTL_KEYS 16384
#define CTL_FLAG1 65536
#define CTL_FLAG2 131072
#define CTL_GU 262144
#define TAG_INC 0x9E3779B9u

#define P1_STG 49152
#define P1_BUF(b) ((b) == 0 ? 98304 : ((b) - 1) * P1_STG)
#define P2_STG 32768
#define LDS_W 98304
#define LDS_MISC 155648
#define LDS_TOTAL 163840

__global__ __launch_bounds__(512, 2) void fnet_moe_kernel(const u16* x, const u16* Wsr, const u16* Wsi, const u16* Dr,
                                                          const u16* Di, const u16* wr, const u16* wg, const u16* wu,
                                                          const u16* wd, u16* out, char* ws) {
  __shared__ __attribute__((aligned(1024))) char smem[LDS_TOTAL];
  float* stat_mb = (float*)(smem + LDS_MISC);
  float* stat_ri = stat_mb + 64;
  float* stat_ws = stat_mb + 128;
  u32* misc = (u32*)(smem + LDS_MISC + 1024);
  const int b = blockIdx.x, xcd = b & 7, slot = b >> 3;
  const int Qd = xcd & 3, part = Qd >> 1, sh = Qd & 1;
  const int tid = threadIdx.x, w = __builtin_amdgcn_readfirstlane(tid >> 6), l = tid & 63;
  const int c = l & 15, q = l >> 4;
  char* ctl = ws + WS_CTL;
  u16* abt = (u16*)(ws + WS_ABT);
  u16* PQ = (u16*)(ws + WS_PQ);
  u16* hnbuf = (u16*)(ws + WS_HN);
  const size_t PS = (size_t)S_LEN * D_DIM;
  const int nt1 = (xcd >> 2) * 2 + (slot >> 4), ks1 = slot & 15;
  const int mt = slot >> 2, nt2 = slot & 3, mtile = (xcd >> 2) * 8 + mt;
  const bool is_p5 = slot >= 28;
  STAMP(0,0);

  if (w >= 4) {
    const int lw = w - 4, lt = tid - 256;
    const u16* Wp = part ? Di : Dr;
    const u16* Wsx = part ? Wsi : Wsr;
    u32 pf0, pf1 = 0, pf2, pf3, pf4;
    const char* pA = (const char*)(Wsx + (size_t)((xcd >> 2) * 1024 + slot * 32 + (lt >> 3)) * S_LEN + sh * 1024) + (lt & 7) * 128;
    {
      const int s0 = sh * 1024 + ks1 * 64;
      {
        const char* p = (const char*)(x + (size_t)(sh * 1024 + slot * 32 + (l >> 1)) * D_DIM) + (lw * 2 + (l & 1)) * 128;
        asm volatile("global_load_dword %0, %1, off" : "=v"(pf0) : "v"(p) : "memory");
      }
      if (lw == 0) {
        const char* p = (const char*)(Wp + (size_t)(xcd >> 2) * 256 * D_DIM) + (size_t)slot * 8192 + l * 128;
        asm volatile("global_load_dword %0, %1, off" : "=v"(pf1) : "v"(p) : "memory");
      }
      const char* xsrc0 = (const char*)(x + (size_t)(s0 + lw * 16 + q) * D_DIM) + ((c ^ (4 * ((2 * lw) & 3) + q)) * 16);
      const char* xsrc1 = (const char*)(x + (size_t)(s0 + lw * 16 + 8 + q) * D_DIM) + ((c ^ (4 * ((2 * lw + 1) & 3) + q)) * 16);
      const char* wsrc = (const char*)(Wp + (size_t)(nt1 * 128 + lw * 32 + q) * D_DIM);
      auto issue = [&](int kq, int buf) {
        char* lb = smem + P1_BUF(buf);
#pragma unroll
        for (int jj = 0; jj < 4; ++jj)
          __builtin_amdgcn_global_load_lds(GLB_PTR(((jj & 2) ? xsrc1 : xsrc0) + (jj & 1) * 4096 + kq * 256),
                                           LDS_PTR(lb + (lw * 4 + jj) * 1024), 16, 0, 0);
#pragma unroll
        for (int jj = 0; jj < 8; ++jj) {
          const int rl = (q + 4 * jj) & 15;
          __builtin_amdgcn_global_load_lds(GLB_PTR(wsrc + jj * 4096 + kq * 256 + ((c ^ rl) * 16)),
                                           LDS_PTR(lb + 16384 + (lw * 8 + jj) * 1024), 16, 0, 0);
        }
      };
      issue(0, 0);
      issue(1, 1);
      wait_vm_bar<12>();
      issue(2, 2);
      wait_vm_bar<12>();
      issue(3, 0);
      wait_vm_bar<12>();
      asm volatile("global_load_dword %0, %1, off nt" : "=v"(pf2) : "v"(pA) : "memory");
      wait_vm_bar<1>();
    }
    const u32 tag = misc[0];
    {
      const int t0 = mtile * 128;
      const char* Abase = (const char*)(Wsx + (size_t)t0 * S_LEN + sh * 1024);
      const char* Bbase = (const char*)abt + (size_t)(Qd * 4 + nt2) * 16 * 16384;
      const u32 av0 = (u32)(l >> 3) * 4096u + (u32)((l & 7) ^ (l >> 4)) * 16u;
      const u32 av1 = (u32)(l >> 3) * 4096u + (u32)((l & 7) ^ (4 + (l >> 4))) * 16u;
      const u32 bv = (u32)l * 16u;
      const int k0 = slot & 7;
      auto kst = [&](int k) { return ((k + k0) & 7) | (k & 8); };
      auto issueA = [&](int k, int buf) {
        const char* ab = Abase + kst(k) * 128;
        char* lb = smem + buf * P2_STG;
#pragma unroll
        for (int jj = 0; jj < 4; ++jj) {
          const int ja = lw * 4 + jj;
          __builtin_amdgcn_global_load_lds(GLB_PTR(ab + (size_t)ja * 32768 + ((jj & 1) ? av1 : av0)), LDS_PTR(lb + ja * 1024), 16, 0, 0);
        }
      };
      auto issueB = [&](int k, int buf) {
        const char* bb = Bbase + (size_t)kst(k) * 16384;
        char* lb = smem + buf * P2_STG;
#pragma unroll
        for (int jj = 0; jj < 4; ++jj) {
          const int ja = lw * 4 + jj;
          __builtin_amdgcn_global_load_lds(GLB_PTR(bb + ja * 1024 + bv), LDS_PTR(lb + 16384 + ja * 1024), 16, 0, 16);
        }
      };
      while (__hip_atomic_load(misc + 4, __ATOMIC_RELAXED, __HIP_MEMORY_SCOPE_WORKGROUP) < 4u) __builtin_amdgcn_s_sleep(1);
      CBAR();
      issueA(0, 0);
      issueA(1, 1);
      issueA(2, 2);
      if (lw == 0) {
        poll_words((const u32*)(ctl + CTL_FLAG1 + (size_t)((Qd * 4 + nt2) * 16 + c) * 128), tag);
        if (l == 0) __hip_atomic_store(misc + 5, 1u, __ATOMIC_RELAXED, __HIP_MEMORY_SCOPE_WORKGROUP);
      } else {
        while (__hip_atomic_load(misc + 5, __ATOMIC_RELAXED, __HIP_MEMORY_SCOPE_WORKGROUP) == 0u) __builtin_amdgcn_s_sleep(1);
      }
      CBAR();
      issueB(0, 0);
      issueB(1, 1);
      wait_vm_bar<4>();
      issueB(2, 2);
      wait_vm_bar<4>();
      issueA(3, 0); issueB(3, 0);
      asm volatile("global_load_dword %0, %1, off nt" : "=v"(pf3) : "v"(pA + 1024) : "memory");
      wait_vm_bar<9>();
      issueA(4, 1); issueB(4, 1);
      wait_vm_bar<9>();
      issueA(5, 2); issueB(5, 2);
      int nbuf = 0;
#pragma unroll 1
      for (int k = 4; k < 10; ++k) {
        wait_vm_bar<8>();
        issueA(k + 2, nbuf); issueB(k + 2, nbuf);
        nbuf = (nbuf == 2) ? 0 : nbuf + 1;
      }
      {
        const char* p;
        if (lt < 128) {
          const int r = slot * 16 + (lt >> 3), mat = (lt >> 2) & 1, ln = lt & 3;
          p = (const char*)((mat ? wu : wg) + (size_t)r * F_DIM) + ((xcd * 448) & ~127) + ln * 128;
        } else {
          int r = lt - 128;
          if (r > 55) r = 55;
          p = (const char*)(wd + (size_t)(slot * 56 + r) * D_DIM) + xcd * 128;
        }
        asm volatile("global_load_dword %0, %1, off nt" : "=v"(pf4) : "v"(p) : "memory");
      }
      wait_vm_bar<9>();
      issueA(12, 0); issueB(12, 0);
      wait_vm_bar<9>();
      issueA(13, 1); issueB(13, 1);
      wait_vm_bar<8>();
      issueA(14, 2); issueB(14, 2);
      wait_vm_bar<8>();
      issueA(15, 0); issueB(15, 0);
      wait_vm_bar<8>();
      if (!is_p5) {
        const int f0 = (xcd * 28 + slot) * 8;
        const u16* src = ((l & 1) ? wu : wg) + (size_t)(lw * 128 + (l >> 1)) * F_DIM + f0;
#pragma unroll
        for (int m = 0; m < 4; ++m)
          __builtin_amdgcn_global_load_lds(GLB_PTR(src + (size_t)m * 32 * F_DIM), LDS_PTR(smem + LDS_W + lw * 4096 + m * 1024), 16, 0, 0);
        wait_vm_bar<4>();
      } else {
        wait_vm_bar<0>();
      }
      asm volatile("s_waitcnt vmcnt(0)" ::"v"(pf0), "v"(pf1), "v"(pf2), "v"(pf3), "v"(pf4) : "memory");
    }
  } else {
    if (w == 0) {
      const u32 e = ld4_dev_wait(ctl + CTL_EPOCH);
      if (l == 0) { misc[0] = e + TAG_INC; misc[1] = 0; misc[2] = 0; misc[3] = 0; misc[4] = 0; misc[5] = 0; }
    }
    {
      const int wsn = w & 1, weh = w >> 1;
      f32x4 acc[2][4] = {};
      f32x4 sx = {}, sq = {}, sw0 = {}, sw1 = {};
      const bf16x8 ones = {0x3f80, 0x3f80, 0x3f80, 0x3f80, 0x3f80, 0x3f80, 0x3f80, 0x3f80};
      const int aoff = (wsn * 32 + 8 * (c >> 2) + (c & 3)) * 256 + weh * 1024;
      const int boff = 16384 + (weh * 64 + c) * 256 + wsn * 8192;
      const int c0 = q ^ c;
      bf16x8 af[2][2], bfr[2][4];
      auto rd = [&](int st, int buf, int kl) {
        const char* lb = smem + P1_BUF(buf) + ((4 * kl) ^ c0) * 16;
#pragma unroll
        for (int i = 0; i < 2; ++i) af[st][i] = *(const bf16x8*)(lb + (aoff ^ (i * 1024)));
#pragma unroll
        for (int j = 0; j < 4; ++j) bfr[st][j] = *(const bf16x8*)(lb + (boff ^ ((j >> 1) * 8192)) + (j & 1) * 4096);
      };
      auto mm = [&](int st) {
        sx = __builtin_amdgcn_mfma_f32_16x16x32_bf16(af[st][0], ones, sx, 0, 0, 0);
        sq = __builtin_amdgcn_mfma_f32_16x16x32_bf16(af[st][0], af[st][0], sq, 0, 0, 0);
        sw0 = __builtin_amdgcn_mfma_f32_16x16x32_bf16(ones, bfr[st][0], sw0, 0, 0, 0);
        sw1 = __builtin_amdgcn_mfma_f32_16x16x32_bf16(ones, bfr[st][1], sw1, 0, 0, 0);
#pragma unroll
        for (int i = 0; i < 2; ++i)
#pragma unroll
          for (int j = 0; j < 4; ++j)
            acc[i][j] = __builtin_amdgcn_mfma_f32_16x16x32_bf16(af[st][i], bfr[st][j], acc[i][j], 0, 0, 0);
      };
#define IL6() do { IL1() IL1() IL1() IL1() IL1() IL1() __builtin_amdgcn_sched_group_barrier(0x008, 6, 0); } while (0)
      auto quarter = [&](int buf) {
        rd(0, buf, 0);
        SB();
        rd(1, buf, 1); mm(0); IL6(); SB();
        rd(0, buf, 2); mm(1); IL6(); SB();
        rd(1, buf, 3); mm(0); IL6(); SB();
        mm(1);
        SB();
      };
      LDS_BARRIER();
      STAMP(0,1);
      quarter(0);
      LDS_BARRIER();
      quarter(1);
      LDS_BARRIER();
      quarter(2);
      LDS_BARRIER();
      quarter(0);
      STAMP(0,2);
      {
        const int r = c & 3;
        const float s1 = r == 0 ? sx[0] : r == 1 ? sx[1] : r == 2 ? sx[2] : sx[3];
        const float s2 = r == 0 ? sq[0] : r == 1 ? sq[1] : r == 2 ? sq[2] : sq[3];
        float mb, ri;
        ln_scalars(s1, s2, mb, ri);
        if (q == (c >> 2)) {
          const int srow = wsn * 32 + 8 * (c >> 2) + 4 * weh + (c & 3);
          stat_mb[srow] = mb; stat_ri[srow] = ri;
        }
        if (q == 0) {
          stat_ws[weh * 64 + (2 * wsn) * 16 + c] = sw0[0];
          stat_ws[weh * 64 + (2 * wsn + 1) * 16 + c] = sw1[0];
        }
      }
      asm volatile("s_waitcnt lgkmcnt(0)" ::: "memory");
      if (l == 0) __hip_atomic_fetch_add(misc + 1, 1u, __ATOMIC_RELAXED, __HIP_MEMORY_SCOPE_WORKGROUP);
      while (__hip_atomic_load(misc + 1, __ATOMIC_RELAXED, __HIP_MEMORY_SCOPE_WORKGROUP) < 4u) { }
      CBAR();
      f32x4 mbv[2], riv[2];
#pragma unroll
      for (int i = 0; i < 2; ++i) {
        mbv[i] = *(const f32x4*)(stat_mb + wsn * 32 + 8 * q + 4 * (i ^ weh));
        riv[i] = *(const f32x4*)(stat_ri + wsn * 32 + 8 * q + 4 * (i ^ weh));
      }
#pragma unroll
      for (int jp = 0; jp < 4; ++jp) {
        const int j = jp ^ (2 * wsn);
        const int e6 = j * 16 + c;
        const int ii = ((e6 >> 5) << 1) | ((e6 >> 2) & 1), qq = (e6 >> 3) & 3, rr = e6 & 3;
        const int p = weh * 64 + 16 * ii + 4 * qq + rr;
        char* dst = (char*)abt + (size_t)((Qd * 4 + nt1) * 16 + ks1) * 16384 + p * 128;
        const int sw = (p >> 1) & 7;
        const float wsj = stat_ws[weh * 64 + j * 16 + c];
        f32x4 v0 = (acc[0][jp] - mbv[0] * wsj) * riv[0];
        f32x4 v1 = (acc[1][jp] - mbv[1] * wsj) * riv[1];
        const u32 a0 = pack_bf16(v0[0], v0[1]), a1 = pack_bf16(v0[2], v0[3]);
        const u32 b0 = pack_bf16(v1[0], v1[1]), b1 = pack_bf16(v1[2], v1[3]);
        u32x4 o0;
        o0[0] = weh ? b0 : a0; o0[1] = weh ? b1 : a1; o0[2] = weh ? a0 : b0; o0[3] = weh ? a1 : b1;
        st16_dev(dst + (((wsn * 4 + q) ^ sw) * 16), o0);
      }
      if (l == 0) __hip_atomic_fetch_add(misc + 4, 1u, __ATOMIC_RELAXED, __HIP_MEMORY_SCOPE_WORKGROUP);
      VM_DRAIN();
      if (l == 0) {
        const u32 n = __hip_atomic_fetch_add(misc + 2, 1u, __ATOMIC_RELAXED, __HIP_MEMORY_SCOPE_WORKGROUP);
        if (n == 3u) st4_dev(ctl + CTL_FLAG1 + (size_t)((Qd * 4 + nt1) * 16 + ks1) * 128, misc[0]);
      }
      STAMP(0,3);
    }
    {
      const int we = w & 1, wt = w >> 1;
      f32x4 acc[4][4] = {};
      const int sw = c >> 1;
      const int ro0 = c * 128 + ((q ^ sw) * 16);
      const int ro1 = c * 128 + (((q + 4) ^ sw) * 16);
      const int aoff = 16384 + we * 8192, boff = wt * 8192;
      bf16x8 a0[4], b0[4], a1[4], b1[4];
      auto rd = [&](bf16x8* af, bf16x8* bf, int buf, int ro) {
        const char* lb = smem + buf * P2_STG + ro;
#pragma unroll
        for (int i = 0; i < 4; ++i) { af[i] = *(const bf16x8*)(lb + aoff + i * 2048); bf[i] = *(const bf16x8*)(lb + boff + i * 2048); }
      };
      auto mm = [&](const bf16x8* af, const bf16x8* bf) {
#pragma unroll
        for (int i = 0; i < 4; ++i)
#pragma unroll
          for (int j = 0; j < 4; ++j)
            acc[i][j] = __builtin_amdgcn_mfma_f32_16x16x32_bf16(af[i], bf[j], acc[i][j], 0, 0, 0);
      };
      LDS_BARRIER();
      STAMP(0,4);
      int buf = 0;
      rd(a0, b0, 0, ro0);
#pragma unroll 1
      for (int k = 0; k < 15; ++k) {
        rd(a1, b1, buf, ro1);
        mm(a0, b0);
        IL();
        SB();
        LDS_BARRIER();
        buf = (buf == 2) ? 0 : buf + 1;
        rd(a0, b0, buf, ro0);
        mm(a1, b1);
        IL();
        SB();
      }
      rd(a1, b1, buf, ro1);
      mm(a0, b0);
      IL();
      SB();
      mm(a1, b1);
      STAMP(0,5);
      const int t0 = mtile * 128;
#pragma unroll
      for (int j = 0; j < 4; ++j) {
        const int t = t0 + wt * 64 + j * 16 + c;
        u16* dst = PQ + (size_t)Qd * PS + (size_t)t * D_DIM + nt2 * 128 + we * 64 + q * 8;
        u32x4 o0, o1;
        o0[0] = pack_bf16(acc[0][j][0], acc[0][j][1]); o0[1] = pack_bf16(acc[0][j][2], acc[0][j][3]);
        o0[2] = pack_bf16(acc[1][j][0], acc[1][j][1]); o0[3] = pack_bf16(acc[1][j][2], acc[1][j][3]);
        o1[0] = pack_bf16(acc[2][j][0], acc[2][j][1]); o1[1] = pack_bf16(acc[2][j][2], acc[2][j][3]);
        o1[2] = pack_bf16(acc[3][j][0], acc[3][j][1]); o1[3] = pack_bf16(acc[3][j][2], acc[3][j][3]);
        st16_dev(dst, o0);
        st16_dev(dst + 32, o1);
      }
      VM_DRAIN();
      if (l == 0) {
        const u32 n = __hip_atomic_fetch_add(misc + 3, 1u, __ATOMIC_RELAXED, __HIP_MEMORY_SCOPE_WORKGROUP);
        if (n == 3u) st4_dev(ctl + CTL_FLAG2 + (size_t)(mtile * 16 + Qd * 4 + nt2) * 128, misc[0]);
      }
      STAMP(0,6);
    }
  }

  const u32 tag = misc[0];
  const int row = xcd * 256 + slot * 8 + w;
  const size_t roff = (size_t)row * D_DIM + l * 8;
  {
    const u32x4 xv = *(const u32x4*)(x + roff);
    const u32x4 wv = *(const u32x4*)(wr + l * 8);
    if (w == 4) poll_words((const u32*)(ctl + CTL_FLAG2 + (size_t)((row >> 7) * 16 + c) * 128), tag);
    wait_vm_bar<0>();
    STAMP(1,0);
    u32x4 p0, p1, q0, q1;
    asm volatile(
        "global_load_dwordx4 %0, %4, off sc1\n\tglobal_load_dwordx4 %1, %5, off sc1\n\t"
        "global_load_dwordx4 %2, %6, off sc1\n\tglobal_load_dwordx4 %3, %7, off sc1\n\ts_waitcnt vmcnt(0)"
        : "=&v"(p0), "=&v"(p1), "=&v"(q0), "=&v"(q1)
        : "v"(PQ + roff), "v"(PQ + PS + roff), "v"(PQ + 2 * PS + roff), "v"(PQ + 3 * PS + roff)
        : "memory");
    STAMP(1,1);
    const u32x4 h = compute_h8(p0, p1, q0, q1, xv);
    float s1 = 0.f, s2 = 0.f;
#pragma unroll
    for (int j = 0; j < 4; ++j) { s1 = dot2bf(h[j], 0x3f803f80u, s1); s2 = dot2bf(h[j], h[j], s2); }
    s1 = wave_sum(s1);
    s2 = wave_sum(s2);
    float mb, rinv;
    ln_scalars(s1, s2, mb, rinv);
    u32x4 hn, o;
    float dot = 0.f;
#pragma unroll
    for (int j = 0; j < 4; ++j) {
      u32 dd = pack_bf16(bf_lo(h[j]) - mb, bf_hi(h[j]) - mb);
      u32 n = pack_bf16(bf_lo(dd) * rinv, bf_hi(dd) * rinv);
      hn[j] = n;
      dot = dot2bf(n, wv[j], dot);
      o[j] = pack_bf16(bf_lo(h[j]) + bf_lo(n), bf_hi(h[j]) + bf_hi(n));
    }
    st16_dev(hnbuf + roff, hn);
    st16_dev(out + roff, o);
    dot = wave_sum(dot);
    if (l == 0) {
      const u32 u = pack_bf16(dot, 0.f) & 0xffffu;
      misc[8 + w] = (u & 0x8000u) ? (~u & 0xffffu) : (u | 0x8000u);
    }
    wait_vm_bar<0>();
    STAMP(1,2);
    if (tid < 2) {
      u32x4 g;
      g[0] = tag;
      g[1] = misc[8 + tid * 4] | (misc[9 + tid * 4] << 16);
      g[2] = misc[10 + tid * 4] | (misc[11 + tid * 4] << 16);
      g[3] = tag;
      st16_dev(ctl + CTL_KEYS + (size_t)(xcd * 32 + slot) * 32 + tid * 16, g);
    }
  }
  const int d0 = (xcd * 4 + (slot - 28)) * 16;
  if (is_p5) {
    const u16* src = wd + (size_t)(w * 224 + (l >> 1)) * D_DIM + d0 + (l & 1) * 8;
#pragma unroll
    for (int m = 0; m < 7; ++m)
      __builtin_amdgcn_global_load_lds(GLB_PTR(src + (size_t)m * 32 * D_DIM), LDS_PTR(smem + LDS_W + w * 7168 + m * 1024), 16, 0, 0);
  }

  u32* cand = (u32*)smem;
  u32* gmax = cand + 64;
  u32* cnt = cand + 72;
  u32* sel = cand + 80;
  char* red = smem + 4096;
  u32 selkey;
  {
    if (tid < 64) cand[tid] = 0;
    if (tid == 0) *cnt = 0;
    u32x4 g;
#pragma unroll 1
    for (int spin = 0; spin < SPIN_CAP; ++spin) {
      g = ld16_dev_wait(ctl + CTL_KEYS + tid * 16);
      if (__all(g[0] == tag && g[3] == tag)) break;
      __builtin_amdgcn_s_sleep(1);
    }
    STAMP(1,3);
    const u32 ib = (u32)(S_LEN - 1 - tid * 4);
    u32 a0 = (g[1] << 16) | ib, a1 = (g[1] & 0xffff0000u) | (ib - 1), a2 = (g[2] << 16) | (ib - 2), a3 = (g[2] & 0xffff0000u) | (ib - 3);
    {
      const u32 m = wave_max_u32(umax(umax(a0, a1), umax(a2, a3)));
      if (l == 0) gmax[w] = m;
    }
    LDS_BARRIER();
    {
      const u32x4 g0 = *(const u32x4*)(gmax), g1 = *(const u32x4*)(gmax + 4);
      const u32 T = umin(umin(umin(g0[0], g0[1]), umin(g0[2], g0[3])), umin(umin(g1[0], g1[1]), umin(g1[2], g1[3])));
      const u32 av[4] = {a0, a1, a2, a3};
#pragma unroll
      for (int j = 0; j < 4; ++j) {
        if (av[j] >= T) {
          const u32 pos = __hip_atomic_fetch_add(cnt, 1u, __ATOMIC_RELAXED, __HIP_MEMORY_SCOPE_WORKGROUP);
          if (pos < 64) cand[pos] = av[j];
        }
      }
    }
    LDS_BARRIER();
    const u32 n = __builtin_amdgcn_readfirstlane(*cnt);
    if (n <= 64) {
      const u32 mine = cand[l];
      int rank = 0;
      for (u32 i0 = 0; i0 < n; i0 += 16) {
#pragma unroll
        for (int i = 0; i < 4; ++i) {
          const u32x4 v = *(const u32x4*)(cand + i0 + i * 4);
          rank += (v[0] > mine) + (v[1] > mine) + (v[2] > mine) + (v[3] > mine);
        }
      }
      if (rank < 8 && (u32)l < n) sel[w * 8 + rank] = mine;
      asm volatile("s_waitcnt lgkmcnt(0)" ::: "memory");
      selkey = sel[w * 8 + (l & 7)];
    } else {
      CE(a0, a1) CE(a2, a3) CE(a0, a2) CE(a1, a3) CE(a1, a2)
      u32 mycand = 0;
#pragma unroll 1
      for (int it = 0; it < 8; ++it) {
        const u32 m = wave_max_u32(a0);
        const bool win = (a0 == m);
        a0 = win ? a1 : a0; a1 = win ? a2 : a1; a2 = win ? a3 : a2; a3 = win ? 0u : a3;
        mycand = (l == it) ? m : mycand;
      }
      LDS_BARRIER();
      if (l < 8) cand[w * 8 + l] = mycand;
      LDS_BARRIER();
      const u32 mine = cand[l];
      int rank = 0;
#pragma unroll 1
      for (int i = 0; i < 16; ++i) {
        const u32x4 v = *(const u32x4*)(cand + i * 4);
        rank += (v[0] > mine) + (v[1] > mine) + (v[2] > mine) + (v[3] > mine);
      }
      if (rank < 8) sel[w * 8 + rank] = mine;
      asm volatile("s_waitcnt lgkmcnt(0)" ::: "memory");
      selkey = sel[w * 8 + (l & 7)];
    }
  }
  STAMP(1,4);
  const int myrow = (S_LEN - 1 - (int)(selkey & 0xffffu)) & (S_LEN - 1);
  if (!is_p5) {
    const int ct = xcd * 28 + slot;
    bf16x8 xf0, xf1;
    {
      const u16* src = hnbuf + (size_t)myrow * D_DIM + w * 64 + q * 8;
      asm volatile("global_load_dwordx4 %0, %2, off sc1\n\tglobal_load_dwordx4 %1, %2, off offset:64 sc1\n\ts_waitcnt vmcnt(0)"
                   : "=&v"(xf0), "=&v"(xf1) : "v"(src) : "memory");
    }
    f32x4 acc = {};
    {
      const char* base = smem + LDS_W + w * 2048 + q * 256 + c * 8;
#pragma unroll
      for (int ks = 0; ks < 2; ++ks) {
        s16x4 lo = __builtin_bit_cast(s16x4, __builtin_amdgcn_ds_read_tr16_b64_v4i16(
                       (__attribute__((address_space(3))) s16x4*)(base + ks * 1024)));
        s16x4 hi = __builtin_bit_cast(s16x4, __builtin_amdgcn_ds_read_tr16_b64_v4i16(
                       (__attribute__((address_space(3))) s16x4*)(base + ks * 1024 + 128)));
        bf16x8 a = {lo[0], lo[1], lo[2], lo[3], hi[0], hi[1], hi[2], hi[3]};
        acc = __builtin_amdgcn_mfma_f32_16x16x32_bf16(a, ks ? xf1 : xf0, acc, 0, 0, 0);
      }
    }
    *(f32x4*)(red + w * 1024 + l * 16) = acc;
    LDS_BARRIER();
    if (w == 0) {
      if (l < 32 && c < 8) {
        f32x4 g = *(const f32x4*)(red + l * 16);
        f32x4 u = *(const f32x4*)(red + (l + 32) * 16);
#pragma unroll
        for (int ww = 1; ww < 8; ++ww) {
          g += *(const f32x4*)(red + ww * 1024 + l * 16);
          u += *(const f32x4*)(red + ww * 1024 + (l + 32) * 16);
        }
        float o[4];
#pragma unroll
        for (int r = 0; r < 4; ++r) {
          const float gp = rbf(g[r]);
          const float sg = rbf(gp / (1.0f + __expf(-gp)));
          o[r] = sg * rbf(u[r]);
        }
        u32x4 ov = {tag, pack_bf16(o[0], o[1]), pack_bf16(o[2], o[3]), tag};
        st16_dev(ctl + CTL_GU + (size_t)ct * 256 + c * 32 + q * 16, ov);
      }
    }
    STAMP(1,5);
    return;
  }

  {
    u32x2 hnv = {0, 0}, e0 = {0, 0}, e1 = {0, 0}, e2 = {0, 0}, e3 = {0, 0}, xv2 = {0, 0};
    const size_t eoff = (size_t)myrow * D_DIM + d0 + q * 4;
    if (w == 0) {
      xv2 = *(const u32x2*)(x + eoff);
      asm volatile(
          "global_load_dwordx2 %0, %5, off sc1\n\tglobal_load_dwordx2 %1, %6, off sc1\n\tglobal_load_dwordx2 %2, %7, off sc1\n\t"
          "global_load_dwordx2 %3, %8, off sc1\n\tglobal_load_dwordx2 %4, %9, off sc1\n\ts_waitcnt vmcnt(0)"
          : "=&v"(hnv), "=&v"(e0), "=&v"(e1), "=&v"(e2), "=&v"(e3)
          : "v"(hnbuf + eoff), "v"(PQ + eoff), "v"(PQ + PS + eoff), "v"(PQ + 2 * PS + eoff), "v"(PQ + 3 * PS + eoff)
          : "memory");
    }
    bf16x8 gf[7];
    {
      const char* src = ctl + CTL_GU + (size_t)(w * 28 + q) * 256 + (c & 7) * 32;
#pragma unroll 1
      for (int spin = 0; spin < SPIN_CAP; ++spin) {
        u32x4 g[14];
        asm volatile(
            "global_load_dwordx4 %0, %14, off sc1\n\tglobal_load_dwordx4 %1, %14, off offset:16 sc1\n\t"
            "global_load_dwordx4 %2, %14, off offset:1024 sc1\n\tglobal_load_dwordx4 %3, %14, off offset:1040 sc1\n\t"
            "global_load_dwordx4 %4, %14, off offset:2048 sc1\n\tglobal_load_dwordx4 %5, %14, off offset:2064 sc1\n\t"
            "global_load_dwordx4 %6, %14, off offset:3072 sc1\n\tglobal_load_dwordx4 %7, %14, off offset:3088 sc1\n\t"
            "global_load_dwordx4 %8, %15, off sc1\n\tglobal_load_dwordx4 %9, %15, off offset:16 sc1\n\t"
            "global_load_dwordx4 %10, %15, off offset:1024 sc1\n\tglobal_load_dwordx4 %11, %15, off offset:1040 sc1\n\t"
            "global_load_dwordx4 %12, %15, off offset:2048 sc1\n\tglobal_load_dwordx4 %13, %15, off offset:2064 sc1\n\t"
            "s_waitcnt vmcnt(0)"
            : "=&v"(g[0]), "=&v"(g[1]), "=&v"(g[2]), "=&v"(g[3]), "=&v"(g[4]), "=&v"(g[5]), "=&v"(g[6]), "=&v"(g[7]),
              "=&v"(g[8]), "=&v"(g[9]), "=&v"(g[10]), "=&v"(g[11]), "=&v"(g[12]), "=&v"(g[13])
            : "v"(src), "v"(src + 4096) : "memory");
        bool ok = true;
#pragma unroll
        for (int i = 0; i < 14; ++i) ok = ok && (g[i][0] == tag) && (g[i][3] == tag);
#pragma unroll
        for (int ks = 0; ks < 7; ++ks) {
          const u32x4 f = {g[2 * ks][1], g[2 * ks][2], g[2 * ks + 1][1], g[2 * ks + 1][2]};
          gf[ks] = __builtin_bit_cast(bf16x8, f);
        }
        if (__all(ok)) break;
        __builtin_amdgcn_s_sleep(2);
      }
    }
    STAMP(1,5);
    f32x4 acc = {};
    {
      const char* base = smem + LDS_W + w * 7168 + q * 256 + c * 8;
#pragma unroll
      for (int ks = 0; ks < 7; ++ks) {
        s16x4 lo = __builtin_bit_cast(s16x4, __builtin_amdgcn_ds_read_tr16_b64_v4i16(
                       (__attribute__((address_space(3))) s16x4*)(base + ks * 1024)));
        s16x4 hi = __builtin_bit_cast(s16x4, __builtin_amdgcn_ds_read_tr16_b64_v4i16(
                       (__attribute__((address_space(3))) s16x4*)(base + ks * 1024 + 128)));
        bf16x8 a = {lo[0], lo[1], lo[2], lo[3], hi[0], hi[1], hi[2], hi[3]};
        acc = __builtin_amdgcn_mfma_f32_16x16x32_bf16(a, gf[ks], acc, 0, 0, 0);
      }
    }
    *(f32x4*)(red + w * 1024 + l * 16) = acc;
    LDS_BARRIER();
    if (b == 255 && tid == 64) st4_dev(ctl + CTL_EPOCH, tag);
    if (w == 0 && c < 8) {
      f32x4 y = *(const f32x4*)(red + l * 16);
#pragma unroll
      for (int ww = 1; ww < 8; ++ww) y += *(const f32x4*)(red + ww * 1024 + l * 16);
      const u32 key = selkey >> 16;
      const u32 u = (key & 0x8000u) ? (key & 0x7fffu) : (~key & 0xffffu);
      const float sg = rbf(1.0f / (1.0f + __expf(-__uint_as_float(u << 16))));
      const u32x4 z = {0, 0, 0, 0};
      u32x4 p0 = z, p1 = z, q0 = z, q1 = z, xx = z;
      p0[0] = e0[0]; p0[1] = e0[1]; p1[0] = e1[0]; p1[1] = e1[1];
      q0[0] = e2[0]; q0[1] = e2[1]; q1[0] = e3[0]; q1[1] = e3[1]; xx[0] = xv2[0]; xx[1] = xv2[1];
      const u32x4 hh = compute_h8(p0, p1, q0, q1, xx);
      float hn4[4] = {bf_lo(hnv[0]), bf_hi(hnv[0]), bf_lo(hnv[1]), bf_hi(hnv[1])};
      float h4[4] = {bf_lo(hh[0]), bf_hi(hh[0]), bf_lo(hh[1]), bf_hi(hh[1])};
      float o[4];
#pragma unroll
      for (int r = 0; r < 4; ++r) {
        const float yy = rbf(rbf(y[r]) * sg);
        o[r] = h4[r] + rbf(hn4[r] + yy);
      }
      u32x2 ov = {pack_bf16(o[0], o[1]), pack_bf16(o[2], o[3])};
      st8_dev(out + eoff, ov);
    }
    STAMP(1,6);
  }
}

extern "C" void kernel_launch(void* const* d_in, const int* in_sizes, int n_in, void* d_out, int out_size, void* d_ws,
                              size_t ws_size, hipStream_t stream) {
  fnet_moe_kernel<<<256, 512, 0, stream>>>((const u16*)d_in[0], (const u16*)d_in[1], (const u16*)d_in[2], (const u16*)d_in[3],
                                           (const u16*)d_in[4], (const u16*)d_in[6], (const u16*)d_in[7], (const u16*)d_in[8],
                                           (const u16*)d_in[9], (u16*)d_out, (char*)d_ws);
}
```

```cpp
#include <hip/hip_runtime.h>
#include <hip/hip_bf16.h>
#include <stdint.h>

typedef unsigned short u16;
typedef unsigned int u32;
typedef __attribute__((ext_vector_type(8))) short bf16x8;
typedef __attribute__((ext_vector_type(4))) short s16x4;
typedef __attribute__((ext_vector_type(4))) float f32x4;
typedef __attribute__((ext_vector_type(2))) float f32x2;
typedef __attribute__((ext_vector_type(2))) __bf16 bf16x2_t;
typedef __attribute__((ext_vector_type(4))) u32 u32x4;
typedef __attribute__((ext_vector_type(2))) u32 u32x2;

#define STAMP(K,I)

#define S_LEN 2048
#define D_DIM 512
#define F_DIM 1792

#define LDS_PTR(p) ((__attribute__((address_space(3))) void*)(p))
#define GLB_PTR(p) ((const __attribute__((address_space(1))) void*)(p))
#define SB() __builtin_amdgcn_sched_barrier(0)
#define CBAR() asm volatile("" ::: "memory")
#define IL1() __builtin_amdgcn_sched_group_barrier(0x008, 1, 0); __builtin_amdgcn_sched_group_barrier(0x100, 1, 0);
#define IL() do { IL1() IL1() IL1() IL1() IL1() IL1() IL1() IL1() __builtin_amdgcn_sched_group_barrier(0x008, 8, 0); } while (0)
#define LDS_BARRIER() asm volatile("s_waitcnt lgkmcnt(0)\n\ts_barrier" ::: "memory")

__device__ __forceinline__ u32 pack_bf16(float lo, float hi) {
  f32x2 v = {lo, hi};
  bf16x2_t b = __builtin_convertvector(v, bf16x2_t);
  return __builtin_bit_cast(u32, b);
}
__device__ __forceinline__ float bf_lo(u32 u) { return __uint_as_float(u << 16); }
__device__ __forceinline__ float bf_hi(u32 u) { return __uint_as_float(u & 0xffff0000u); }
__device__ __forceinline__ float rbf(float x) { return bf_lo(pack_bf16(x, 0.f)); }
#define ST_POLICY 0
__device__ __forceinline__ void store16_wt(void* p, u32x4 v) {
  *(u32x4*)p = v;
}
__device__ __forceinline__ float dot2bf(u32 a, u32 b, float acc) {
  return __builtin_amdgcn_fdot2_f32_bf16(__builtin_bit_cast(bf16x2_t, a), __builtin_bit_cast(bf16x2_t, b), acc, false);
}

template <int CTRL>
__device__ __forceinline__ float dppf(float x) {
  return __builtin_bit_cast(float, __builtin_amdgcn_mov_dpp(__builtin_bit_cast(int, x), CTRL, 0xf, 0xf, true));
}
template <int CTRL>
__device__ __forceinline__ u32 dppu(u32 x) {
  return (u32)__builtin_amdgcn_mov_dpp((int)x, CTRL, 0xf, 0xf, true);
}
__device__ __forceinline__ float xrow_sum(float x) {
  auto s = __builtin_amdgcn_permlane16_swap(__float_as_uint(x), __float_as_uint(x), false, false);
  x = __uint_as_float(s[0]) + __uint_as_float(s[1]);
  auto t = __builtin_amdgcn_permlane32_swap(__float_as_uint(x), __float_as_uint(x), false, false);
  return __uint_as_float(t[0]) + __uint_as_float(t[1]);
}
__device__ __forceinline__ float wave_sum(float x) {
  x += dppf<0xB1>(x);
  x += dppf<0x4E>(x);
  x += dppf<0x141>(x);
  x += dppf<0x140>(x);
  return xrow_sum(x);
}
__device__ __forceinline__ u32 umax(u32 a, u32 b) { return a > b ? a : b; }
__device__ __forceinline__ u32 umin(u32 a, u32 b) { return a < b ? a : b; }
__device__ __forceinline__ u32 wave_max_u32(u32 x) {
  x = umax(x, dppu<0xB1>(x));
  x = umax(x, dppu<0x4E>(x));
  x = umax(x, dppu<0x141>(x));
  x = umax(x, dppu<0x140>(x));
  auto s = __builtin_amdgcn_permlane16_swap(x, x, false, false);
  x = umax(s[0], s[1]);
  auto t = __builtin_amdgcn_permlane32_swap(x, x, false, false);
  x = umax(t[0], t[1]);
  return x;
}
__device__ __forceinline__ void ln_scalars(float s1, float s2, float& mb, float& rinv) {
  const float mean = s1 * (1.0f / D_DIM);
  const float var = fmaxf(s2 * (1.0f / D_DIM) - mean * mean, 0.f);
  mb = rbf(mean);
  const float vb = rbf(var);
  const float sd = rbf(__builtin_amdgcn_sqrtf(rbf(vb + 1e-5f)));
  rinv = __builtin_amdgcn_rcpf(sd);
}

template <int N> __device__ __forceinline__ void wait_vm_bar() { asm volatile("s_waitcnt vmcnt(%0) lgkmcnt(0)\n\ts_barrier" ::"n"(N) : "memory"); }
#define VM_DRAIN() asm volatile("s_waitcnt vmcnt(0)" ::: "memory")

__device__ __forceinline__ void st16_dev(void* p, u32x4 v) {
  asm volatile("global_store_dwordx4 %0, %1, off sc1\n\ts_nop 1" ::"v"(p), "v"(v) : "memory");
}
__device__ __forceinline__ void st8_dev(void* p, u32x2 v) {
  asm volatile("global_store_dwordx2 %0, %1, off sc1\n\ts_nop 1" ::"v"(p), "v"(v) : "memory");
}
__device__ __forceinline__ void st4_dev(void* p, u32 v) {
  asm volatile("global_store_dword %0, %1, off sc1" ::"v"(p), "v"(v) : "memory");
}
typedef __attribute__((address_space(1))) const u32x4 gc_u32x4;
__device__ __forceinline__ u32 ld4_dev_wait(const void* p) {
  u32 v;
  asm volatile("global_load_dword %0, %1, off sc1\n\ts_waitcnt vmcnt(0)" : "=&v"(v) : "v"(p) : "memory");
  return v;
}
__device__ __forceinline__ u32x4 ld16_dev_wait(const void* p) {
  u32x4 v;
  asm volatile("global_load_dwordx4 %0, %1, off sc1\n\ts_waitcnt vmcnt(0)" : "=&v"(v) : "v"(p) : "memory");
  return v;
}
#define SPIN_CAP 20000
__device__ __forceinline__ void poll_words(const u32* p, u32 tag) {
#pragma unroll 1
  for (int spin = 0; spin < SPIN_CAP; ++spin) {
    const u32 v = ld4_dev_wait(p);
    if (__all(v == tag)) break;
    __builtin_amdgcn_s_sleep(1);
  }
}

__device__ __forceinline__ u32x4 compute_h8(const u32x4& p0, const u32x4& p1, const u32x4& q0, const u32x4& q1, const u32x4& xv) {
  u32x4 h;
#pragma unroll
  for (int j = 0; j < 4; ++j) {
    u32 p = pack_bf16(bf_lo(p0[j]) + bf_lo(p1[j]), bf_hi(p0[j]) + bf_hi(p1[j]));
    u32 qq = pack_bf16(bf_lo(q0[j]) + bf_lo(q1[j]), bf_hi(q0[j]) + bf_hi(q1[j]));
    u32 y = pack_bf16(bf_lo(p) - bf_lo(qq), bf_hi(p) - bf_hi(qq));
    h[j] = pack_bf16(bf_lo(xv[j]) + bf_lo(y), bf_hi(xv[j]) + bf_hi(y));
  }
  return h;
}
__device__ __forceinline__ void row_h_hn(const u16* pq, const u32x4& xv, u32x4& h, u32x4& hn) {
  const size_t PS = (size_t)S_LEN * D_DIM;
  u32x4 p0, p1, q0, q1;
  asm volatile(
      "global_load_dwordx4 %0, %4, off sc1\n\tglobal_load_dwordx4 %1, %5, off sc1\n\t"
      "global_load_dwordx4 %2, %6, off sc1\n\tglobal_load_dwordx4 %3, %7, off sc1\n\ts_waitcnt vmcnt(0)"
      : "=&v"(p0), "=&v"(p1), "=&v"(q0), "=&v"(q1)
      : "v"(pq), "v"(pq + PS), "v"(pq + 2 * PS), "v"(pq + 3 * PS)
      : "memory");
  h = compute_h8(p0, p1, q0, q1, xv);
  float s1 = 0.f, s2 = 0.f;
#pragma unroll
  for (int j = 0; j < 4; ++j) { s1 = dot2bf(h[j], 0x3f803f80u, s1); s2 = dot2bf(h[j], h[j], s2); }
  s1 = wave_sum(s1);
  s2 = wave_sum(s2);
  float mb, rinv;
  ln_scalars(s1, s2, mb, rinv);
#pragma unroll
  for (int j = 0; j < 4; ++j) {
    u32 dd = pack_bf16(bf_lo(h[j]) - mb, bf_hi(h[j]) - mb);
    hn[j] = pack_bf16(bf_lo(dd) * rinv, bf_hi(dd) * rinv);
  }
}
#define CE(a, b) { u32 _hi = umax(a, b), _lo = umin(a, b); a = _hi; b = _lo; }

#define WS_ABT 0
#define WS_PQ (4u << 20)
#define WS_HN (12u << 20)
#define WS_CTL (14u << 20)
#define CTL_EPOCH 0
#define CTL_KEYS 16384
#define CTL_FLAG1 65536
#define CTL_FLAG2 131072
#define CTL_FLAG3 196608
#define CTL_GU 262144
#define TAG_INC 0x9E3779B9u

#define P1_STG 49152
#define P1_BUF(b) ((b) == 0 ? 98304 : ((b) - 1) * P1_STG)
#define P2_STG 32768
#define LDS_W 98304
#define LDS_MISC 155648
#define LDS_TOTAL 163840

__global__ __launch_bounds__(512, 2) void fnet_moe_kernel(const u16* x, const u16* Wsr, const u16* Wsi, const u16* Dr,
                                                          const u16* Di, const u16* wr, const u16* wg, const u16* wu,
                                                          const u16* wd, u16* out, char* ws) {
  __shared__ __attribute__((aligned(1024))) char smem[LDS_TOTAL];
  float* stat_mb = (float*)(smem + LDS_MISC);
  float* stat_ri = stat_mb + 64;
  float* stat_ws = stat_mb + 128;
  u32* misc = (u32*)(smem + LDS_MISC + 1024);
  const int b = blockIdx.x, xcd = b & 7, slot = b >> 3;
  const int Qd = xcd & 3, part = Qd >> 1, sh = Qd & 1;
  const int tid = threadIdx.x, w = __builtin_amdgcn_readfirstlane(tid >> 6), l = tid & 63;
  const int c = l & 15, q = l >> 4;
  char* ctl = ws + WS_CTL;
  u16* abt = (u16*)(ws + WS_ABT);
  u16* PQ = (u16*)(ws + WS_PQ);
  const size_t PS = (size_t)S_LEN * D_DIM;
  const int nt1 = (xcd >> 2) * 2 + (slot >> 4), ks1 = slot & 15;
  const int mt = slot >> 2, nt2 = slot & 3, mtile = (xcd >> 2) * 8 + mt;
  const bool is_p5 = slot >= 28;
  STAMP(0,0);

  if (w >= 4) {
    const int lw = w - 4, lt = tid - 256;
    const u16* Wp = part ? Di : Dr;
    const u16* Wsx = part ? Wsi : Wsr;
    u32 pf0, pf1 = 0, pf2 = 0, pf3, pf4;
    const char* pA = (const char*)(Wsx + (size_t)((xcd >> 2) * 1024 + slot * 32 + lw * 8 + ((l >> 2) & 7)) * S_LEN + sh * 1024) +
                     ((l & 3) + 4 * (l >> 5)) * 128;
    u32 pf5 = 0;
    {
      const int s0 = sh * 1024 + ks1 * 64;
      {
        const char* p = (const char*)(x + (size_t)(sh * 1024 + slot * 32 + (l >> 1)) * D_DIM) + (lw * 2 + (l & 1)) * 128;
        asm volatile("global_load_dword %0, %1, off" : "=v"(pf0) : "v"(p) : "memory");
      }
      if (lw == 0) {
        const char* p = (const char*)(Wp + (size_t)(xcd >> 2) * 256 * D_DIM) + (size_t)slot * 8192 + l * 128;
        asm volatile("global_load_dword %0, %1, off" : "=v"(pf1) : "v"(p) : "memory");
      }
      const char* xsrc0 = (const char*)(x + (size_t)(s0 + lw * 16 + q) * D_DIM) + ((c ^ (4 * ((2 * lw) & 3) + q)) * 16);
      const char* xsrc1 = (const char*)(x + (size_t)(s0 + lw * 16 + 8 + q) * D_DIM) + ((c ^ (4 * ((2 * lw + 1) & 3) + q)) * 16);
      const char* wsrc = (const char*)(Wp + (size_t)(nt1 * 128 + lw * 32 + q) * D_DIM);
      auto issue = [&](int kq, int buf) {
        char* lb = smem + P1_BUF(buf);
#pragma unroll
        for (int jj = 0; jj < 4; ++jj)
          __builtin_amdgcn_global_load_lds(GLB_PTR(((jj & 2) ? xsrc1 : xsrc0) + (jj & 1) * 4096 + kq * 256),
                                           LDS_PTR(lb + (lw * 4 + jj) * 1024), 16, 0, 0);
#pragma unroll
        for (int jj = 0; jj < 8; ++jj) {
          const int rl = (q + 4 * jj) & 15;
          __builtin_amdgcn_global_load_lds(GLB_PTR(wsrc + jj * 4096 + kq * 256 + ((c ^ rl) * 16)),
                                           LDS_PTR(lb + 16384 + (lw * 8 + jj) * 1024), 16, 0, 0);
        }
      };
      issue(0, 0);
      issue(1, 1);
      wait_vm_bar<12>();
      issue(2, 2);
      wait_vm_bar<12>();
      issue(3, 0);
      wait_vm_bar<12>();
      if (l < 32) asm volatile("global_load_dword %0, %1, off nt" : "=v"(pf2) : "v"(pA) : "memory");
      wait_vm_bar<1>();
    }
    const u32 tag = misc[0];
    {
      const int t0 = mtile * 128;
      const char* Abase = (const char*)(Wsx + (size_t)t0 * S_LEN + sh * 1024);
      const char* Bbase = (const char*)abt + (size_t)(Qd * 4 + nt2) * 16 * 16384;
      const u32 av0 = (u32)(l >> 3) * 4096u + (u32)((l & 7) ^ (l >> 4)) * 16u;
      const u32 av1 = (u32)(l >> 3) * 4096u + (u32)((l & 7) ^ (4 + (l >> 4))) * 16u;
      const u32 bv = (u32)l * 16u;
      const int k0 = slot & 7;
      auto kst = [&](int k) { return k < 8 ? (((k + k0) & 3) | (k & 4)) : (((k + k0) & 7) | 8); };
      auto issueA = [&](int k, int buf) {
        const char* ab = Abase + kst(k) * 128;
        char* lb = smem + buf * P2_STG;
#pragma unroll
        for (int jj = 0; jj < 4; ++jj) {
          const int ja = lw * 4 + jj;
          __builtin_amdgcn_global_load_lds(GLB_PTR(ab + (size_t)ja * 32768 + ((jj & 1) ? av1 : av0)), LDS_PTR(lb + ja * 1024), 16, 0, 0);
        }
      };
      auto issueB = [&](int k, int buf) {
        const char* bb = Bbase + (size_t)kst(k) * 16384;
        char* lb = smem + buf * P2_STG;
#pragma unroll
        for (int jj = 0; jj < 4; ++jj) {
          const int ja = lw * 4 + jj;
          __builtin_amdgcn_global_load_lds(GLB_PTR(bb + ja * 1024 + bv), LDS_PTR(lb + 16384 + ja * 1024), 16, 0, 16);
        }
      };
      while (__hip_atomic_load(misc + 4, __ATOMIC_RELAXED, __HIP_MEMORY_SCOPE_WORKGROUP) == 0u) __builtin_amdgcn_s_sleep(1);
      CBAR();
      issueA(0, 0);
      issueA(1, 1);
      issueA(2, 2);

      if (lw == 0) {
        poll_words((const u32*)(ctl + CTL_FLAG1 + (size_t)((Qd * 4 + nt2) * 16 + c) * 128), tag);
        if (l == 0) __hip_atomic_store(misc + 5, 1u, __ATOMIC_RELAXED, __HIP_MEMORY_SCOPE_WORKGROUP);
      } else {
        while (__hip_atomic_load(misc + 5, __ATOMIC_RELAXED, __HIP_MEMORY_SCOPE_WORKGROUP) == 0u) __builtin_amdgcn_s_sleep(1);
      }
      CBAR();
      issueB(0, 0);
      issueB(1, 1);
      wait_vm_bar<4>();
      issueB(2, 2);
      if (l >= 32) asm volatile("global_load_dword %0, %1, off nt" : "=v"(pf5) : "v"(pA) : "memory");
      wait_vm_bar<5>();
      issueA(3, 0); issueB(3, 0);
      asm volatile("global_load_dword %0, %1, off nt" : "=v"(pf3) : "v"(pA + 1024) : "memory");
      wait_vm_bar<10>();
      issueA(4, 1); issueB(4, 1);
      wait_vm_bar<9>();
      issueA(5, 2); issueB(5, 2);
      int nbuf = 0;
#pragma unroll 1
      for (int k = 4; k < 10; ++k) {
        wait_vm_bar<8>();
        issueA(k + 2, nbuf); issueB(k + 2, nbuf);
        nbuf = (nbuf == 2) ? 0 : nbuf + 1;
      }
      {
        const char* p;
        if (lt < 128) {
          const int r = slot * 16 + (lt >> 3), mat = (lt >> 2) & 1, ln = lt & 3;
          p = (const char*)((mat ? wu : wg) + (size_t)r * F_DIM) + ((xcd * 448) & ~127) + ln * 128;
        } else {
          int r = lt - 128;
          if (r > 55) r = 55;
          p = (const char*)(wd + (size_t)(slot * 56 + r) * D_DIM) + xcd * 128;
        }
        asm volatile("global_load_dword %0, %1, off" : "=v"(pf4) : "v"(p) : "memory");
      }
      wait_vm_bar<9>();
      issueA(12, 0); issueB(12, 0);
      wait_vm_bar<9>();
      issueA(13, 1); issueB(13, 1);
      wait_vm_bar<8>();
      issueA(14, 2); issueB(14, 2);
      wait_vm_bar<8>();
      issueA(15, 0); issueB(15, 0);
      wait_vm_bar<8>();
      wait_vm_bar<0>();
      asm volatile("" ::"v"(pf0), "v"(pf1), "v"(pf2), "v"(pf3), "v"(pf4), "v"(pf5));
      while (__hip_atomic_load(misc + 6, __ATOMIC_RELAXED, __HIP_MEMORY_SCOPE_WORKGROUP) == 0u) __builtin_amdgcn_s_sleep(1);
      CBAR();
      if (!is_p5) {
        const int f0 = (xcd * 28 + slot) * 8;
        const u16* src = ((l & 1) ? wu : wg) + (size_t)(lw * 128 + (l >> 1)) * F_DIM + f0;
#pragma unroll
        for (int m = 0; m < 4; ++m)
          __builtin_amdgcn_global_load_lds(GLB_PTR(src + (size_t)m * 32 * F_DIM), LDS_PTR(smem + LDS_W + lw * 4096 + m * 1024), 16, 0, 0);
      }
    }
  } else {
    if (w == 1) {
      unsigned long long pc;
      u32 len;
      asm volatile("s_getpc_b64 %0\nfnet_code_begin:\n\ts_mov_b32 %1, fnet_code_end-fnet_code_begin" : "=s"(pc), "=s"(len));
      const u32 off = (u32)(slot + 32 * l) * 128u;
      if (off < len) { const u32 v = *(const u32*)((const char*)pc + off); asm volatile("" ::"v"(v)); }
    }
    if (w == 0) {
      const u32 e = ld4_dev_wait(ctl + CTL_EPOCH);
      if (l == 0) { misc[0] = e + TAG_INC; misc[1] = 0; misc[2] = 0; misc[3] = 0; misc[4] = 0; misc[5] = 0; misc[6] = 0; misc[7] = 0; }
    }
    {
      const int wsn = w & 1, weh = w >> 1;
      f32x4 acc[2][4] = {};
      f32x4 sx = {}, sq = {}, sw0 = {}, sw1 = {};
      const bf16x8 ones = {0x3f80, 0x3f80, 0x3f80, 0x3f80, 0x3f80, 0x3f80, 0x3f80, 0x3f80};
      const int aoff = (wsn * 32 + 8 * (c >> 2) + (c & 3)) * 256 + weh * 1024;
      const int boff = 16384 + (weh * 64 + c) * 256 + wsn * 8192;
      const int c0 = q ^ c;
      bf16x8 af[2][2], bfr[2][4];
      auto rd = [&](int st, int buf, int kl) {
        const char* lb = smem + P1_BUF(buf) + ((4 * kl) ^ c0) * 16;
#pragma unroll
        for (int i = 0; i < 2; ++i) af[st][i] = *(const bf16x8*)(lb + (aoff ^ (i * 1024)));
#pragma unroll
        for (int j = 0; j < 4; ++j) bfr[st][j] = *(const bf16x8*)(lb + (boff ^ ((j >> 1) * 8192)) + (j & 1) * 4096);
      };
      auto mm = [&](int st) {
        sx = __builtin_amdgcn_mfma_f32_16x16x32_bf16(af[st][0], ones, sx, 0, 0, 0);
        sq = __builtin_amdgcn_mfma_f32_16x16x32_bf16(af[st][0], af[st][0], sq, 0, 0, 0);
        sw0 = __builtin_amdgcn_mfma_f32_16x16x32_bf16(ones, bfr[st][0], sw0, 0, 0, 0);
        sw1 = __builtin_amdgcn_mfma_f32_16x16x32_bf16(ones, bfr[st][1], sw1, 0, 0, 0);
#pragma unroll
        for (int i = 0; i < 2; ++i)
#pragma unroll
          for (int j = 0; j < 4; ++j)
            acc[i][j] = __builtin_amdgcn_mfma_f32_16x16x32_bf16(af[st][i], bfr[st][j], acc[i][j], 0, 0, 0);
      };
#define IL6() do { IL1() IL1() IL1() IL1() IL1() IL1() __builtin_amdgcn_sched_group_barrier(0x008, 6, 0); } while (0)
      auto quarter = [&](int buf) {
        rd(0, buf, 0);
        SB();
        rd(1, buf, 1); mm(0); IL6(); SB();
        rd(0, buf, 2); mm(1); IL6(); SB();
        rd(1, buf, 3); mm(0); IL6(); SB();
        mm(1);
        SB();
      };
      LDS_BARRIER();
      STAMP(0,1);
      quarter(0);
      LDS_BARRIER();
      quarter(1);
      LDS_BARRIER();
      quarter(2);
      LDS_BARRIER();
      quarter(0);
      STAMP(0,2);
      {
        const int r = c & 3;
        const float s1 = r == 0 ? sx[0] : r == 1 ? sx[1] : r == 2 ? sx[2] : sx[3];
        const float s2 = r == 0 ? sq[0] : r == 1 ? sq[1] : r == 2 ? sq[2] : sq[3];
        float mb, ri;
        ln_scalars(s1, s2, mb, ri);
        if (q == (c >> 2)) {
          const int srow = wsn * 32 + 8 * (c >> 2) + 4 * weh + (c & 3);
          stat_mb[srow] = mb; stat_ri[srow] = ri;
        }
        if (q == 0) {
          stat_ws[weh * 64 + (2 * wsn) * 16 + c] = sw0[0];
          stat_ws[weh * 64 + (2 * wsn + 1) * 16 + c] = sw1[0];
        }
      }
      asm volatile("s_waitcnt lgkmcnt(0)" ::: "memory");
      if (l == 0) __hip_atomic_fetch_add(misc + 1, 1u, __ATOMIC_RELAXED, __HIP_MEMORY_SCOPE_WORKGROUP);
      while (__hip_atomic_load(misc + 1, __ATOMIC_RELAXED, __HIP_MEMORY_SCOPE_WORKGROUP) < 4u) { }
      CBAR();
      f32x4 mbv[2], riv[2];
#pragma unroll
      for (int i = 0; i < 2; ++i) {
        mbv[i] = *(const f32x4*)(stat_mb + wsn * 32 + 8 * q + 4 * (i ^ weh));
        riv[i] = *(const f32x4*)(stat_ri + wsn * 32 + 8 * q + 4 * (i ^ weh));
      }
#pragma unroll
      for (int jp = 0; jp < 4; ++jp) {
        const int j = jp ^ (2 * wsn);
        const int e6 = j * 16 + c;
        const int ii = ((e6 >> 5) << 1) | ((e6 >> 2) & 1), qq = (e6 >> 3) & 3, rr = e6 & 3;
        const int p = weh * 64 + 16 * ii + 4 * qq + rr;
        char* dst = (char*)abt + (size_t)((Qd * 4 + nt1) * 16 + ks1) * 16384 + p * 128;
        const int sw = (p >> 1) & 7;
        const float wsj = stat_ws[weh * 64 + j * 16 + c];
        f32x4 v0 = (acc[0][jp] - mbv[0] * wsj) * riv[0];
        f32x4 v1 = (acc[1][jp] - mbv[1] * wsj) * riv[1];
        const u32 a0 = pack_bf16(v0[0], v0[1]), a1 = pack_bf16(v0[2], v0[3]);
        const u32 b0 = pack_bf16(v1[0], v1[1]), b1 = pack_bf16(v1[2], v1[3]);
        u32x4 o0;
        o0[0] = weh ? b0 : a0; o0[1] = weh ? b1 : a1; o0[2] = weh ? a0 : b0; o0[3] = weh ? a1 : b1;
        st16_dev(dst + (((wsn * 4 + q) ^ sw) * 16), o0);
      }
      VM_DRAIN();
      if (l == 0) {
        const u32 n = __hip_atomic_fetch_add(misc + 2, 1u, __ATOMIC_RELAXED, __HIP_MEMORY_SCOPE_WORKGROUP);
        if (n == 3u) {
          st4_dev(ctl + CTL_FLAG1 + (size_t)((Qd * 4 + nt1) * 16 + ks1) * 128, misc[0]);
          __hip_atomic_store(misc + 4, 1u, __ATOMIC_RELAXED, __HIP_MEMORY_SCOPE_WORKGROUP);
        }
      }
      STAMP(0,3);
    }
    {
      const int we = w & 1, wt = w >> 1;
      f32x4 acc[4][4] = {};
      const int sw = c >> 1;
      const int ro0 = c * 128 + ((q ^ sw) * 16);
      const int ro1 = c * 128 + (((q + 4) ^ sw) * 16);
      const int aoff = 16384 + we * 8192, boff = wt * 8192;
      bf16x8 a0[4], b0[4], a1[4], b1[4];
      auto rd = [&](bf16x8* af, bf16x8* bf, int buf, int ro) {
        const char* lb = smem + buf * P2_STG + ro;
#pragma unroll
        for (int i = 0; i < 4; ++i) { af[i] = *(const bf16x8*)(lb + aoff + i * 2048); bf[i] = *(const bf16x8*)(lb + boff + i * 2048); }
      };
      auto mm = [&](const bf16x8* af, const bf16x8* bf) {
#pragma unroll
        for (int i = 0; i < 4; ++i)
#pragma unroll
          for (int j = 0; j < 4; ++j)
            acc[i][j] = __builtin_amdgcn_mfma_f32_16x16x32_bf16(af[i], bf[j], acc[i][j], 0, 0, 0);
      };
      LDS_BARRIER();
      STAMP(0,4);
      int buf = 0;
      rd(a0, b0, 0, ro0);
#pragma unroll 1
      for (int k = 0; k < 15; ++k) {
        rd(a1, b1, buf, ro1);
        mm(a0, b0);
        IL();
        SB();
        LDS_BARRIER();
        buf = (buf == 2) ? 0 : buf + 1;
        rd(a0, b0, buf, ro0);
        mm(a1, b1);
        IL();
        SB();
      }
      rd(a1, b1, buf, ro1);
      mm(a0, b0);
      IL();
      SB();
      mm(a1, b1);
      STAMP(0,5);
      const int t0 = mtile * 128;
#pragma unroll
      for (int j = 0; j < 4; ++j) {
        const int t = t0 + wt * 64 + j * 16 + c;
        u16* dst = PQ + (size_t)Qd * PS + (size_t)t * D_DIM + nt2 * 128 + we * 64 + q * 8;
        u32x4 o0, o1;
        o0[0] = pack_bf16(acc[0][j][0], acc[0][j][1]); o0[1] = pack_bf16(acc[0][j][2], acc[0][j][3]);
        o0[2] = pack_bf16(acc[1][j][0], acc[1][j][1]); o0[3] = pack_bf16(acc[1][j][2], acc[1][j][3]);
        o1[0] = pack_bf16(acc[2][j][0], acc[2][j][1]); o1[1] = pack_bf16(acc[2][j][2], acc[2][j][3]);
        o1[2] = pack_bf16(acc[3][j][0], acc[3][j][1]); o1[3] = pack_bf16(acc[3][j][2], acc[3][j][3]);
        st16_dev(dst, o0);
        st16_dev(dst + 32, o1);
      }
      VM_DRAIN();
      if (l == 0) {
        const u32 n = __hip_atomic_fetch_add(misc + 3, 1u, __ATOMIC_RELAXED, __HIP_MEMORY_SCOPE_WORKGROUP);
        if (n == 3u) {
          st4_dev(ctl + CTL_FLAG2 + (size_t)(mtile * 16 + Qd * 4 + nt2) * 128, misc[0]);
          __hip_atomic_store(misc + 6, 1u, __ATOMIC_RELAXED, __HIP_MEMORY_SCOPE_WORKGROUP);
        }
      }
      STAMP(0,6);
    }
  }

  const u32 tag = misc[0];
  const u32x4 wv = *(const u32x4*)(wr + l * 8);
  {
    const int row = xcd * 256 + slot * 8 + w;
    const size_t roff = (size_t)row * D_DIM + l * 8;
    const u32x4 xv = *(const u32x4*)(x + roff);
    if (w == 4) poll_words((const u32*)(ctl + CTL_FLAG2 + (size_t)((row >> 7) * 16 + c) * 128), tag);
    wait_vm_bar<0>();
    STAMP(1,0);
    u32x4 h, hn;
    row_h_hn(PQ + roff, xv, h, hn);
    STAMP(1,1);
    float dot = 0.f;
#pragma unroll
    for (int j = 0; j < 4; ++j) dot = dot2bf(hn[j], wv[j], dot);
    dot = wave_sum(dot);
    if (l == 0) {
      const u32 u = pack_bf16(dot, 0.f) & 0xffffu;
      misc[8 + w] = (u & 0x8000u) ? (~u & 0xffffu) : (u | 0x8000u);
    }
    LDS_BARRIER();
    if (tid < 2) {
      u32x4 g;
      g[0] = tag;
      g[1] = misc[8 + tid * 4] | (misc[9 + tid * 4] << 16);
      g[2] = misc[10 + tid * 4] | (misc[11 + tid * 4] << 16);
      g[3] = tag;
      st16_dev(ctl + CTL_KEYS + (size_t)(xcd * 32 + slot) * 32 + tid * 16, g);
    }
    STAMP(1,2);
    u32x4 o;
#pragma unroll
    for (int j = 0; j < 4; ++j) o[j] = pack_bf16(bf_lo(h[j]) + bf_lo(hn[j]), bf_hi(h[j]) + bf_hi(hn[j]));
    st16_dev(out + roff, o);
    VM_DRAIN();
    if (l == 0) {
      const u32 n = __hip_atomic_fetch_add(misc + 7, 1u, __ATOMIC_RELAXED, __HIP_MEMORY_SCOPE_WORKGROUP);
      if (n == 7u) st4_dev(ctl + CTL_FLAG3 + (size_t)(xcd * 32 + slot) * 128, tag);
    }
  }
  const int d0 = (xcd * 4 + (slot - 28)) * 16;
  if (is_p5) {
    const u16* src = wd + (size_t)(w * 224 + (l >> 1)) * D_DIM + d0 + (l & 1) * 8;
#pragma unroll
    for (int m = 0; m < 7; ++m)
      __builtin_amdgcn_global_load_lds(GLB_PTR(src + (size_t)m * 32 * D_DIM), LDS_PTR(smem + LDS_W + w * 7168 + m * 1024), 16, 0, 0);
  }

  u32* cand = (u32*)smem;
  u32* gmax = cand + 64;
  u32* cnt = cand + 72;
  u32* sel = cand + 80;
  char* red = smem + 4096;
  char* hnsel = smem + 16384;
  char* hsel = smem + 32768;
  u32 selkey;
  {
    if (tid < 64) cand[tid] = 0;
    if (tid == 0) *cnt = 0;
    u32x4 g;
#pragma unroll 1
    for (int spin = 0; spin < SPIN_CAP; ++spin) {
      g = ld16_dev_wait(ctl + CTL_KEYS + tid * 16);
      if (__all(g[0] == tag && g[3] == tag)) break;
      __builtin_amdgcn_s_sleep(2);
    }
    STAMP(1,3);
    const u32 ib = (u32)(S_LEN - 1 - tid * 4);
    u32 a0 = (g[1] << 16) | ib, a1 = (g[1] & 0xffff0000u) | (ib - 1), a2 = (g[2] << 16) | (ib - 2), a3 = (g[2] & 0xffff0000u) | (ib - 3);
    {
      const u32 m = wave_max_u32(umax(umax(a0, a1), umax(a2, a3)));
      if (l == 0) gmax[w] = m;
    }
    LDS_BARRIER();
    {
      const u32x4 g0 = *(const u32x4*)(gmax), g1 = *(const u32x4*)(gmax + 4);
      const u32 T = umin(umin(umin(g0[0], g0[1]), umin(g0[2], g0[3])), umin(umin(g1[0], g1[1]), umin(g1[2], g1[3])));
      const u32 av[4] = {a0, a1, a2, a3};
#pragma unroll
      for (int j = 0; j < 4; ++j) {
        if (av[j] >= T) {
          const u32 pos = __hip_atomic_fetch_add(cnt, 1u, __ATOMIC_RELAXED, __HIP_MEMORY_SCOPE_WORKGROUP);
          if (pos < 64) cand[pos] = av[j];
        }
      }
    }
    LDS_BARRIER();
    const u32 n = __builtin_amdgcn_readfirstlane(*cnt);
    if (n <= 64) {
      const u32 mine = cand[l];
      int rank = 0;
      for (u32 i0 = 0; i0 < n; i0 += 16) {
#pragma unroll
        for (int i = 0; i < 4; ++i) {
          const u32x4 v = *(const u32x4*)(cand + i0 + i * 4);
          rank += (v[0] > mine) + (v[1] > mine) + (v[2] > mine) + (v[3] > mine);
        }
      }
      if (rank < 8 && (u32)l < n) sel[w * 8 + rank] = mine;
      asm volatile("s_waitcnt lgkmcnt(0)" ::: "memory");
      selkey = sel[w * 8 + (l & 7)];
    } else {
      CE(a0, a1) CE(a2, a3) CE(a0, a2) CE(a1, a3) CE(a1, a2)
      u32 mycand = 0;
#pragma unroll 1
      for (int it = 0; it < 8; ++it) {
        const u32 m = wave_max_u32(a0);
        const bool win = (a0 == m);
        a0 = win ? a1 : a0; a1 = win ? a2 : a1; a2 = win ? a3 : a2; a3 = win ? 0u : a3;
        mycand = (l == it) ? m : mycand;
      }
      LDS_BARRIER();
      if (l < 8) cand[w * 8 + l] = mycand;
      LDS_BARRIER();
      const u32 mine = cand[l];
      int rank = 0;
#pragma unroll 1
      for (int i = 0; i < 16; ++i) {
        const u32x4 v = *(const u32x4*)(cand + i * 4);
        rank += (v[0] > mine) + (v[1] > mine) + (v[2] > mine) + (v[3] > mine);
      }
      if (rank < 8) sel[w * 8 + rank] = mine;
      asm volatile("s_waitcnt lgkmcnt(0)" ::: "memory");
      selkey = sel[w * 8 + (l & 7)];
    }
  }
  STAMP(1,4);
  const int myrow = (S_LEN - 1 - (int)(selkey & 0xffffu)) & (S_LEN - 1);
  {
    const int trow = __builtin_amdgcn_readlane(myrow, w);
    const size_t roff = (size_t)trow * D_DIM + l * 8;
    const u32x4 xv = *(const u32x4*)(x + roff);
    u32x4 h, hn;
    row_h_hn(PQ + roff, xv, h, hn);
    *(u32x4*)(hnsel + w * 1040 + l * 16) = hn;
    *(u32x4*)(hsel + w * 1040 + l * 16) = h;
  }
  LDS_BARRIER();
  if (!is_p5) {
    const int ct = xcd * 28 + slot;
    f32x4 acc = {};
    {
      const char* base = smem + LDS_W + w * 2048 + q * 256 + c * 8;
#pragma unroll
      for (int ks = 0; ks < 2; ++ks) {
        const bf16x8 xf = *(const bf16x8*)(hnsel + (c & 7) * 1040 + w * 128 + ks * 64 + q * 16);
        s16x4 lo = __builtin_bit_cast(s16x4, __builtin_amdgcn_ds_read_tr16_b64_v4i16(
                       (__attribute__((address_space(3))) s16x4*)(base + ks * 1024)));
        s16x4 hi = __builtin_bit_cast(s16x4, __builtin_amdgcn_ds_read_tr16_b64_v4i16(
                       (__attribute__((address_space(3))) s16x4*)(base + ks * 1024 + 128)));
        bf16x8 a = {lo[0], lo[1], lo[2], lo[3], hi[0], hi[1], hi[2], hi[3]};
        acc = __builtin_amdgcn_mfma_f32_16x16x32_bf16(a, xf, acc, 0, 0, 0);
      }
    }
    *(f32x4*)(red + w * 1024 + l * 16) = acc;
    LDS_BARRIER();
    if (w == 0) {
      if (l < 32 && c < 8) {
        f32x4 g = *(const f32x4*)(red + l * 16);
        f32x4 u = *(const f32x4*)(red + (l + 32) * 16);
#pragma unroll
        for (int ww = 1; ww < 8; ++ww) {
          g += *(const f32x4*)(red + ww * 1024 + l * 16);
          u += *(const f32x4*)(red + ww * 1024 + (l + 32) * 16);
        }
        float o[4];
#pragma unroll
        for (int r = 0; r < 4; ++r) {
          const float gp = rbf(g[r]);
          const float sg = rbf(gp / (1.0f + __expf(-gp)));
          o[r] = sg * rbf(u[r]);
        }
        u32x4 ov = {tag, pack_bf16(o[0], o[1]), pack_bf16(o[2], o[3]), tag};
        st16_dev(ctl + CTL_GU + (size_t)ct * 256 + (q * 8 + c) * 16, ov);
      }
    }
    STAMP(1,5);
    return;
  }

  {
    if (w == 0) poll_words((const u32*)(ctl + CTL_FLAG3 + (size_t)(myrow >> 3) * 128), tag);
    bf16x8 gf[7];
    {
      const char* src = ctl + CTL_GU + (size_t)(w * 28 + q) * 256 + c * 16;
#pragma unroll 1
      for (int spin = 0; spin < SPIN_CAP; ++spin) {
        const u32x4 g = ld16_dev_wait(src);
        if (__all(g[0] == tag && g[3] == tag)) break;
        __builtin_amdgcn_s_sleep(2);
      }
#pragma unroll 1
      for (int spin = 0; spin < SPIN_CAP; ++spin) {
        u32x4 g[7];
        asm volatile(
            "global_load_dwordx4 %0, %7, off sc1\n\tglobal_load_dwordx4 %1, %7, off offset:1024 sc1\n\t"
            "global_load_dwordx4 %2, %7, off offset:2048 sc1\n\tglobal_load_dwordx4 %3, %7, off offset:3072 sc1\n\t"
            "global_load_dwordx4 %4, %8, off sc1\n\tglobal_load_dwordx4 %5, %8, off offset:1024 sc1\n\t"
            "global_load_dwordx4 %6, %8, off offset:2048 sc1\n\ts_waitcnt vmcnt(0)"
            : "=&v"(g[0]), "=&v"(g[1]), "=&v"(g[2]), "=&v"(g[3]), "=&v"(g[4]), "=&v"(g[5]), "=&v"(g[6])
            : "v"(src), "v"(src + 4096) : "memory");
        bool ok = true;
#pragma unroll
        for (int i = 0; i < 7; ++i) ok = ok && (g[i][0] == tag) && (g[i][3] == tag);
#pragma unroll
        for (int ks = 0; ks < 7; ++ks) {
          const u32x4 f = {g[ks][1], g[ks][2], dppu<0x128>(g[ks][1]), dppu<0x128>(g[ks][2])};
          gf[ks] = __builtin_bit_cast(bf16x8, f);
        }
        if (__all(ok)) break;
        __builtin_amdgcn_s_sleep(1);
      }
    }
    STAMP(1,5);
    f32x4 acc = {};
    {
      const char* base = smem + LDS_W + w * 7168 + q * 256 + c * 8;
#pragma unroll
      for (int ks = 0; ks < 7; ++ks) {
        s16x4 lo = __builtin_bit_cast(s16x4, __builtin_amdgcn_ds_read_tr16_b64_v4i16(
                       (__attribute__((address_space(3))) s16x4*)(base + ks * 1024)));
        s16x4 hi = __builtin_bit_cast(s16x4, __builtin_amdgcn_ds_read_tr16_b64_v4i16(
                       (__attribute__((address_space(3))) s16x4*)(base + ks * 1024 + 128)));
        bf16x8 a = {lo[0], lo[1], lo[2], lo[3], hi[0], hi[1], hi[2], hi[3]};
        acc = __builtin_amdgcn_mfma_f32_16x16x32_bf16(a, gf[ks], acc, 0, 0, 0);
      }
    }
    *(f32x4*)(red + w * 1024 + l * 16) = acc;
    LDS_BARRIER();
    if (b == 255 && tid == 64) st4_dev(ctl + CTL_EPOCH, tag);
    if (w == 0 && c < 8) {
      f32x4 y = *(const f32x4*)(red + l * 16);
#pragma unroll
      for (int ww = 1; ww < 8; ++ww) y += *(const f32x4*)(red + ww * 1024 + l * 16);
      const u32 key = selkey >> 16;
      const u32 u = (key & 0x8000u) ? (key & 0x7fffu) : (~key & 0xffffu);
      const float sg = rbf(1.0f / (1.0f + __expf(-__uint_as_float(u << 16))));
      const u32x2 hnv = *(const u32x2*)(hnsel + c * 1040 + (d0 + q * 4) * 2);
      const u32x2 hh = *(const u32x2*)(hsel + c * 1040 + (d0 + q * 4) * 2);
      float hn4[4] = {bf_lo(hnv[0]), bf_hi(hnv[0]), bf_lo(hnv[1]), bf_hi(hnv[1])};
      float h4[4] = {bf_lo(hh[0]), bf_hi(hh[0]), bf_lo(hh[1]), bf_hi(hh[1])};
      float o[4];
#pragma unroll
      for (int r = 0; r < 4; ++r) {
        const float yy = rbf(rbf(y[r]) * sg);
        o[r] = h4[r] + rbf(hn4[r] + yy);
      }
      u32x2 ov = {pack_bf16(o[0], o[1]), pack_bf16(o[2], o[3])};
      st8_dev(out + (size_t)myrow * D_DIM + d0 + q * 4, ov);
    }
    STAMP(1,6);
    asm volatile("fnet_code_end:");
  }
}

extern "C" void kernel_launch(void* const* d_in, const int* in_sizes, int n_in, void* d_out, int out_size, void* d_ws,
                              size_t ws_size, hipStream_t stream) {
  fnet_moe_kernel<<<256, 512, 0, stream>>>((const u16*)d_in[0], (const u16*)d_in[1], (const u16*)d_in[2], (const u16*)d_in[3],
                                           (const u16*)d_in[4], (const u16*)d_in[6], (const u16*)d_in[7], (const u16*)d_in[8],
                                           (const u16*)d_in[9], (u16*)d_out, (char*)d_ws);
}
```

```cpp
#include <hip/hip_runtime.h>
#include <hip/hip_bf16.h>
#include <stdint.h>

typedef unsigned short u16;
typedef unsigned int u32;
typedef __attribute__((ext_vector_type(8))) short bf16x8;
typedef __attribute__((ext_vector_type(4))) short s16x4;
typedef __attribute__((ext_vector_type(4))) float f32x4;
typedef __attribute__((ext_vector_type(2))) float f32x2;
typedef __attribute__((ext_vector_type(2))) __bf16 bf16x2_t;
typedef __attribute__((ext_vector_type(4))) u32 u32x4;
typedef __attribute__((ext_vector_type(2))) u32 u32x2;

#define STAMP(K,I)

#define EPI_LDS
#define S_LEN 2048
#define D_DIM 512
#define F_DIM 1792

#define LDS_PTR(p) ((__attribute__((address_space(3))) void*)(p))
#define GLB_PTR(p) ((const __attribute__((address_space(1))) void*)(p))
#define SB() __builtin_amdgcn_sched_barrier(0)
#define CBAR() asm volatile("" ::: "memory")
#define IL1() __builtin_amdgcn_sched_group_barrier(0x008, 1, 0); __builtin_amdgcn_sched_group_barrier(0x100, 1, 0);
#define IL() do { IL1() IL1() IL1() IL1() IL1() IL1() IL1() IL1() __builtin_amdgcn_sched_group_barrier(0x008, 8, 0); } while (0)
#define LDS_BARRIER() asm volatile("s_waitcnt lgkmcnt(0)\n\ts_barrier" ::: "memory")

__device__ __forceinline__ u32 pack_bf16(float lo, float hi) {
  f32x2 v = {lo, hi};
  bf16x2_t b = __builtin_convertvector(v, bf16x2_t);
  return __builtin_bit_cast(u32, b);
}
__device__ __forceinline__ float bf_lo(u32 u) { return __uint_as_float(u << 16); }
__device__ __forceinline__ float bf_hi(u32 u) { return __uint_as_float(u & 0xffff0000u); }
__device__ __forceinline__ float rbf(float x) { return bf_lo(pack_bf16(x, 0.f)); }
#define ST_POLICY 0
__device__ __forceinline__ void store16_wt(void* p, u32x4 v) {
  *(u32x4*)p = v;
}
__device__ __forceinline__ float dot2bf(u32 a, u32 b, float acc) {
  return __builtin_amdgcn_fdot2_f32_bf16(__builtin_bit_cast(bf16x2_t, a), __builtin_bit_cast(bf16x2_t, b), acc, false);
}

template <int CTRL>
__device__ __forceinline__ float dppf(float x) {
  return __builtin_bit_cast(float, __builtin_amdgcn_mov_dpp(__builtin_bit_cast(int, x), CTRL, 0xf, 0xf, true));
}
template <int CTRL>
__device__ __forceinline__ u32 dppu(u32 x) {
  return (u32)__builtin_amdgcn_mov_dpp((int)x, CTRL, 0xf, 0xf, true);
}
__device__ __forceinline__ float xrow_sum(float x) {
  auto s = __builtin_amdgcn_permlane16_swap(__float_as_uint(x), __float_as_uint(x), false, false);
  x = __uint_as_float(s[0]) + __uint_as_float(s[1]);
  auto t = __builtin_amdgcn_permlane32_swap(__float_as_uint(x), __float_as_uint(x), false, false);
  return __uint_as_float(t[0]) + __uint_as_float(t[1]);
}
__device__ __forceinline__ float wave_sum(float x) {
  x += dppf<0xB1>(x);
  x += dppf<0x4E>(x);
  x += dppf<0x141>(x);
  x += dppf<0x140>(x);
  return xrow_sum(x);
}
__device__ __forceinline__ u32 umax(u32 a, u32 b) { return a > b ? a : b; }
__device__ __forceinline__ u32 umin(u32 a, u32 b) { return a < b ? a : b; }
__device__ __forceinline__ u32 wave_max_u32(u32 x) {
  x = umax(x, dppu<0xB1>(x));
  x = umax(x, dppu<0x4E>(x));
  x = umax(x, dppu<0x141>(x));
  x = umax(x, dppu<0x140>(x));
  auto s = __builtin_amdgcn_permlane16_swap(x, x, false, false);
  x = umax(s[0], s[1]);
  auto t = __builtin_amdgcn_permlane32_swap(x, x, false, false);
  x = umax(t[0], t[1]);
  return x;
}
__device__ __forceinline__ void ln_scalars(float s1, float s2, float& mb, float& rinv) {
  const float mean = s1 * (1.0f / D_DIM);
  const float var = fmaxf(s2 * (1.0f / D_DIM) - mean * mean, 0.f);
  mb = rbf(mean);
  const float vb = rbf(var);
  const float sd = rbf(__builtin_amdgcn_sqrtf(rbf(vb + 1e-5f)));
  rinv = __builtin_amdgcn_rcpf(sd);
}

template <int N> __device__ __forceinline__ void wait_vm_bar() { asm volatile("s_waitcnt vmcnt(%0) lgkmcnt(0)\n\ts_barrier" ::"n"(N) : "memory"); }
#define VM_DRAIN() asm volatile("s_waitcnt vmcnt(0)" ::: "memory")

__device__ __forceinline__ void st16_dev(void* p, u32x4 v) {
  asm volatile("global_store_dwordx4 %0, %1, off sc1\n\ts_nop 1" ::"v"(p), "v"(v) : "memory");
}
__device__ __forceinline__ void st8_dev(void* p, u32x2 v) {
  asm volatile("global_store_dwordx2 %0, %1, off sc1\n\ts_nop 1" ::"v"(p), "v"(v) : "memory");
}
__device__ __forceinline__ void st4_dev(void* p, u32 v) {
  asm volatile("global_store_dword %0, %1, off sc1" ::"v"(p), "v"(v) : "memory");
}
typedef __attribute__((address_space(1))) const u32x4 gc_u32x4;
__device__ __forceinline__ u32 ld4_dev_wait(const void* p) {
  u32 v;
  asm volatile("global_load_dword %0, %1, off sc1\n\ts_waitcnt vmcnt(0)" : "=&v"(v) : "v"(p) : "memory");
  return v;
}
__device__ __forceinline__ u32x4 ld16_dev_wait(const void* p) {
  u32x4 v;
  asm volatile("global_load_dwordx4 %0, %1, off sc1\n\ts_waitcnt vmcnt(0)" : "=&v"(v) : "v"(p) : "memory");
  return v;
}
#define SPIN_CAP 20000
__device__ __forceinline__ void poll_words(const u32* p, u32 tag) {
#pragma unroll 1
  for (int spin = 0; spin < SPIN_CAP; ++spin) {
    const u32 v = ld4_dev_wait(p);
    if (__all(v == tag)) break;
    __builtin_amdgcn_s_sleep(1);
  }
}

__device__ __forceinline__ u32x4 compute_h8(const u32x4& p0, const u32x4& p1, const u32x4& q0, const u32x4& q1, const u32x4& xv) {
  u32x4 h;
#pragma unroll
  for (int j = 0; j < 4; ++j) {
    u32 p = pack_bf16(bf_lo(p0[j]) + bf_lo(p1[j]), bf_hi(p0[j]) + bf_hi(p1[j]));
    u32 qq = pack_bf16(bf_lo(q0[j]) + bf_lo(q1[j]), bf_hi(q0[j]) + bf_hi(q1[j]));
    u32 y = pack_bf16(bf_lo(p) - bf_lo(qq), bf_hi(p) - bf_hi(qq));
    h[j] = pack_bf16(bf_lo(xv[j]) + bf_lo(y), bf_hi(xv[j]) + bf_hi(y));
  }
  return h;
}
__device__ __forceinline__ void row_h_hn(const u16* pq, const u32x4& xv, u32x4& h, u32x4& hn) {
  const size_t PS = (size_t)S_LEN * D_DIM;
  u32x4 p0, p1, q0, q1;
  asm volatile(
      "global_load_dwordx4 %0, %4, off sc1\n\tglobal_load_dwordx4 %1, %5, off sc1\n\t"
      "global_load_dwordx4 %2, %6, off sc1\n\tglobal_load_dwordx4 %3, %7, off sc1\n\ts_waitcnt vmcnt(0)"
      : "=&v"(p0), "=&v"(p1), "=&v"(q0), "=&v"(q1)
      : "v"(pq), "v"(pq + PS), "v"(pq + 2 * PS), "v"(pq + 3 * PS)
      : "memory");
  h = compute_h8(p0, p1, q0, q1, xv);
  float s1 = 0.f, s2 = 0.f;
#pragma unroll
  for (int j = 0; j < 4; ++j) { s1 = dot2bf(h[j], 0x3f803f80u, s1); s2 = dot2bf(h[j], h[j], s2); }
  s1 = wave_sum(s1);
  s2 = wave_sum(s2);
  float mb, rinv;
  ln_scalars(s1, s2, mb, rinv);
#pragma unroll
  for (int j = 0; j < 4; ++j) {
    u32 dd = pack_bf16(bf_lo(h[j]) - mb, bf_hi(h[j]) - mb);
    hn[j] = pack_bf16(bf_lo(dd) * rinv, bf_hi(dd) * rinv);
  }
}
#define CE(a, b) { u32 _hi = umax(a, b), _lo = umin(a, b); a = _hi; b = _lo; }

#define WS_ABT 0
#define WS_PQ (4u << 20)
#define WS_HN (12u << 20)
#define WS_CTL (14u << 20)
#define CTL_EPOCH 0
#define CTL_KEYS 16384
#define CTL_FLAG1 65536
#define CTL_FLAG2 131072
#define CTL_FLAG3 196608
#define CTL_GU 262144
#define TAG_INC 0x9E3779B9u

#define P1_W 65536
#define P1_NST 5
#define P2_STG 32768
#define LDS_W 98304
#define LDS_MISC 155648
#define LDS_TOTAL 163840

__global__ __launch_bounds__(512, 2) void fnet_moe_kernel(const u16* x, const u16* Wsr, const u16* Wsi, const u16* Dr,
                                                          const u16* Di, const u16* wr, const u16* wg, const u16* wu,
                                                          const u16* wd, u16* out, char* ws) {
  __shared__ __attribute__((aligned(1024))) char smem[LDS_TOTAL];
  float* stat_mb = (float*)(smem + LDS_MISC);
  float* stat_ri = stat_mb + 64;
  float* stat_ws = stat_mb + 128;
  u32* misc = (u32*)(smem + LDS_MISC + 1024);
  const int b = blockIdx.x, xcd = b & 7, slot = b >> 3;
  const int Qd = xcd & 3, part = Qd >> 1, sh = Qd & 1;
  const int tid = threadIdx.x, w = __builtin_amdgcn_readfirstlane(tid >> 6), l = tid & 63;
  const int c = l & 15, q = l >> 4;
  char* ctl = ws + WS_CTL;
  u16* abt = (u16*)(ws + WS_ABT);
  u16* PQ = (u16*)(ws + WS_PQ);
  const size_t PS = (size_t)S_LEN * D_DIM;
  const int nt1 = (xcd >> 2) * 2 + (slot >> 4), ks1 = slot & 15;
  const int mt = slot >> 2, nt2 = slot & 3, mtile = (xcd >> 2) * 8 + mt;
  const bool is_p5 = slot >= 28;
  STAMP(0,0);

  if (w >= 4) {
    const int lw = w - 4, lt = tid - 256;
    const u16* Wp = part ? Di : Dr;
    const u16* Wsx = part ? Wsi : Wsr;
    u32 pf0 = 0, pf1 = 0, pf2 = 0, pf3, pf4;
    const char* pA = (const char*)(Wsx + (size_t)((xcd >> 2) * 1024 + slot * 32 + lw * 8 + ((l >> 2) & 7)) * S_LEN + sh * 1024) +
                     ((l & 3) + 4 * (l >> 5)) * 128;
    u32 pf5 = 0;
    {
      const int s0 = sh * 1024 + ks1 * 64;
      if (lw == 0) {
        const char* p = (const char*)(Wp + (size_t)(xcd >> 2) * 256 * D_DIM) + (size_t)slot * 8192 + l * 128;
        asm volatile("global_load_dword %0, %1, off" : "=v"(pf1) : "v"(p) : "memory");
      }
      const char* xsrc[2];
#pragma unroll
      for (int jj = 0; jj < 2; ++jj) {
        const int p = (lw * 2 + jj) * 8 + (l >> 3);
        const int srow = 32 * (p >> 5) + 8 * ((p >> 2) & 3) + 4 * ((p >> 4) & 1) + (p & 3);
        xsrc[jj] = (const char*)(x + (size_t)(s0 + srow) * D_DIM) + (((l & 7) ^ ((p >> 1) & 7)) * 16);
      }
      const char* wsrc0 = (const char*)(Wp + (size_t)(nt1 * 128 + lw * 32 + (l >> 3)) * D_DIM) + (((l & 7) ^ (l >> 4)) * 16);
      const char* wsrc1 = (const char*)(Wp + (size_t)(nt1 * 128 + lw * 32 + 8 + (l >> 3)) * D_DIM) + (((l & 7) ^ (4 + (l >> 4))) * 16);
      auto issueX = [&](int kq) {
#pragma unroll
        for (int jj = 0; jj < 2; ++jj)
          __builtin_amdgcn_global_load_lds(GLB_PTR(xsrc[jj] + kq * 128), LDS_PTR(smem + kq * 8192 + (lw * 2 + jj) * 1024), 16, 0, 0);
      };
      auto issueW = [&](int kq) {
        char* lb = smem + P1_W + (kq % P1_NST) * 16384;
#pragma unroll
        for (int jj = 0; jj < 4; ++jj)
          __builtin_amdgcn_global_load_lds(GLB_PTR(((jj & 1) ? wsrc1 : wsrc0) + (jj >> 1) * 16384 + kq * 128),
                                           LDS_PTR(lb + (lw * 4 + jj) * 1024), 16, 0, 0);
      };
      issueX(0); issueX(1); issueX(2); issueX(3);
      issueW(0);
      issueW(1);
      wait_vm_bar<4>();
      issueX(4); issueX(5); issueX(6); issueX(7);
      wait_vm_bar<8>();
      issueW(2);
      issueW(3);
      wait_vm_bar<4>();
      issueW(4);
      issueW(5);
      if (l < 32) asm volatile("global_load_dword %0, %1, off nt" : "=v"(pf2) : "v"(pA) : "memory");
      wait_vm_bar<9>();
      issueW(6);
      wait_vm_bar<9>();
      issueW(7);
      wait_vm_bar<9>();
      if (l >= 32) asm volatile("global_load_dword %0, %1, off nt" : "=v"(pf5) : "v"(pA) : "memory");
      wait_vm_bar<5>();
      wait_vm_bar<1>();
    }
    {
      const int t0 = mtile * 128;
      const char* Abase = (const char*)(Wsx + (size_t)t0 * S_LEN + sh * 1024);
      const char* Bbase = (const char*)abt + (size_t)(Qd * 4 + nt2) * 16 * 16384;
      const u32 av0 = (u32)(l >> 3) * 4096u + (u32)((l & 7) ^ (l >> 4)) * 16u;
      const u32 av1 = (u32)(l >> 3) * 4096u + (u32)((l & 7) ^ (4 + (l >> 4))) * 16u;
      const u32 bv = (u32)l * 16u;
#define K0EXPR (slot & 7)
      const int k0 = K0EXPR;
      auto kst = [&](int k) { return ((k + k0) & 7) | (k & 8); };
      auto issueA = [&](int k, int buf) {
        const char* ab = Abase + kst(k) * 128;
        char* lb = smem + buf * P2_STG;
#pragma unroll
        for (int jj = 0; jj < 4; ++jj) {
          const int ja = lw * 4 + jj;
          __builtin_amdgcn_global_load_lds(GLB_PTR(ab + (size_t)ja * 32768 + ((jj & 1) ? av1 : av0)), LDS_PTR(lb + ja * 1024), 16, 0, 0);
        }
      };
      auto issueB = [&](int k, int buf) {
        const char* bb = Bbase + (size_t)kst(k) * 16384;
        char* lb = smem + buf * P2_STG;
#pragma unroll
        for (int jj = 0; jj < 4; ++jj) {
          const int ja = lw * 4 + jj;
          __builtin_amdgcn_global_load_lds(GLB_PTR(bb + ja * 1024 + bv), LDS_PTR(lb + 16384 + ja * 1024), 16, 0, 16);
        }
      };
      while (__hip_atomic_load(misc + 4, __ATOMIC_RELAXED, __HIP_MEMORY_SCOPE_WORKGROUP) == 0u) __builtin_amdgcn_s_sleep(1);
      CBAR();
      const u32 tag = misc[0];
      issueA(0, 0);
      issueA(1, 1);
      issueA(2, 2);

      if (lw == 0) {
        poll_words((const u32*)(ctl + CTL_FLAG1 + (size_t)((Qd * 4 + nt2) * 16 + c) * 128), tag);
        if (l == 0) __hip_atomic_store(misc + 5, 1u, __ATOMIC_RELAXED, __HIP_MEMORY_SCOPE_WORKGROUP);
      } else {
        while (__hip_atomic_load(misc + 5, __ATOMIC_RELAXED, __HIP_MEMORY_SCOPE_WORKGROUP) == 0u) __builtin_amdgcn_s_sleep(1);
      }
      CBAR();
      issueB(0, 0);
      issueB(1, 1);
      wait_vm_bar<4>();
      issueB(2, 2);
      wait_vm_bar<4>();
      issueA(3, 0); issueB(3, 0);
      asm volatile("global_load_dword %0, %1, off nt" : "=v"(pf3) : "v"(pA + 1024) : "memory");
      wait_vm_bar<9>();
      issueA(4, 1); issueB(4, 1);
      wait_vm_bar<9>();
      issueA(5, 2); issueB(5, 2);
      int nbuf = 0;
#pragma unroll 1
      for (int k = 4; k < 10; ++k) {
        wait_vm_bar<8>();
        issueA(k + 2, nbuf); issueB(k + 2, nbuf);
        nbuf = (nbuf == 2) ? 0 : nbuf + 1;
      }
      {
        const char* p;
        if (lt < 128) {
          const int r = slot * 16 + (lt >> 3), mat = (lt >> 2) & 1, ln = lt & 3;
          p = (const char*)((mat ? wu : wg) + (size_t)r * F_DIM) + ((xcd * 448) & ~127) + ln * 128;
        } else {
          int r = lt - 128;
          if (r > 55) r = 55;
          p = (const char*)(wd + (size_t)(slot * 56 + r) * D_DIM) + xcd * 128;
        }
        asm volatile("global_load_dword %0, %1, off" : "=v"(pf4) : "v"(p) : "memory");
      }
      wait_vm_bar<9>();
      issueA(12, 0); issueB(12, 0);
      wait_vm_bar<9>();
      issueA(13, 1); issueB(13, 1);
      wait_vm_bar<8>();
      issueA(14, 2); issueB(14, 2);
      wait_vm_bar<8>();
      issueA(15, 0); issueB(15, 0);
      wait_vm_bar<8>();
      wait_vm_bar<0>();
      asm volatile("" ::"v"(pf0), "v"(pf1), "v"(pf2), "v"(pf3), "v"(pf4), "v"(pf5));
    }
  } else {
    u32 cpf = 0;
    if (w == 1) {
      unsigned long long pc;
      u32 len;
      asm volatile("s_getpc_b64 %0\nfnet_code_begin:\n\ts_mov_b32 %1, fnet_code_end-fnet_code_begin" : "=s"(pc), "=s"(len));
      const u32 off = (u32)(slot + 32 * l) * 128u;
      if (off < len) asm volatile("global_load_dword %0, %1, off" : "=v"(cpf) : "v"((const char*)pc + off) : "memory");
    }
    u32 epoch;
    asm volatile("global_load_dword %0, %1, off sc1" : "=v"(epoch) : "v"(ctl + CTL_EPOCH) : "memory");
    if (tid < 8) misc[tid] = 0;
    {
      const int wsn = w & 1, weh = w >> 1;
      f32x4 acc[2][4] = {};
      f32x4 sx = {}, sq = {}, sw0 = {}, sw1v = {};
      const bf16x8 ones = {0x3f80, 0x3f80, 0x3f80, 0x3f80, 0x3f80, 0x3f80, 0x3f80, 0x3f80};
      const int sw1 = c >> 1;
      const int ro0 = c * 128 + ((q ^ sw1) * 16), ro1 = c * 128 + (((q + 4) ^ sw1) * 16);
      const int aoff = wsn * 4096 + weh * 2048;
      const int boff = P1_W + weh * 8192 + wsn * 4096;
      bf16x8 af[2][2], bfr[2][4];
      auto rd = [&](int st, int kq, int slotw) {
        const char* la = smem + kq * 8192 + (st ? ro1 : ro0);
        const char* lb = smem + slotw * 16384 + (st ? ro1 : ro0);
#pragma unroll
        for (int i = 0; i < 2; ++i) af[st][i] = *(const bf16x8*)(la + (aoff ^ (i * 2048)));
#pragma unroll
        for (int j = 0; j < 4; ++j) bfr[st][j] = *(const bf16x8*)(lb + (boff ^ ((j >> 1) * 4096)) + (j & 1) * 2048);
      };
      auto mm = [&](int st) {
        sx = __builtin_amdgcn_mfma_f32_16x16x32_bf16(af[st][0], ones, sx, 0, 0, 0);
        sq = __builtin_amdgcn_mfma_f32_16x16x32_bf16(af[st][0], af[st][0], sq, 0, 0, 0);
        sw0 = __builtin_amdgcn_mfma_f32_16x16x32_bf16(ones, bfr[st][0], sw0, 0, 0, 0);
        sw1v = __builtin_amdgcn_mfma_f32_16x16x32_bf16(ones, bfr[st][1], sw1v, 0, 0, 0);
#pragma unroll
        for (int i = 0; i < 2; ++i)
#pragma unroll
          for (int j = 0; j < 4; ++j)
            acc[i][j] = __builtin_amdgcn_mfma_f32_16x16x32_bf16(af[st][i], bfr[st][j], acc[i][j], 0, 0, 0);
      };
#define IL6() do { IL1() IL1() IL1() IL1() IL1() IL1() __builtin_amdgcn_sched_group_barrier(0x008, 6, 0); } while (0)
      LDS_BARRIER();
      STAMP(0,1);
      rd(0, 0, 0);
      int stg = 0;
#pragma unroll 1
      for (int k = 0; k < 7; ++k) {
        rd(1, k, stg); mm(0); IL6(); SB();
        LDS_BARRIER();
        stg = (stg + 1 == P1_NST) ? 0 : stg + 1;
        rd(0, k + 1, stg); mm(1); IL6(); SB();
      }
      rd(1, 7, stg); mm(0); IL6(); SB();
      mm(1);
      STAMP(0,2);
      asm volatile("s_waitcnt vmcnt(0)" : "+v"(epoch), "+v"(cpf)::"memory");
      const u32 ctag = epoch + TAG_INC;
      if (tid == 0) misc[0] = ctag;
      {
        const int r = c & 3;
        const float s1 = r == 0 ? sx[0] : r == 1 ? sx[1] : r == 2 ? sx[2] : sx[3];
        const float s2 = r == 0 ? sq[0] : r == 1 ? sq[1] : r == 2 ? sq[2] : sq[3];
        float mb, ri;
        ln_scalars(s1, s2, mb, ri);
        if (q == (c >> 2)) {
          const int srow = wsn * 32 + 8 * (c >> 2) + 4 * weh + (c & 3);
          stat_mb[srow] = mb; stat_ri[srow] = ri;
        }
        if (q == 0) {
          stat_ws[weh * 64 + (2 * wsn) * 16 + c] = sw0[0];
          stat_ws[weh * 64 + (2 * wsn + 1) * 16 + c] = sw1v[0];
        }
      }
      asm volatile("s_waitcnt lgkmcnt(0)" ::: "memory");
      if (l == 0) __hip_atomic_fetch_add(misc + 1, 1u, __ATOMIC_RELAXED, __HIP_MEMORY_SCOPE_WORKGROUP);
      while (__hip_atomic_load(misc + 1, __ATOMIC_RELAXED, __HIP_MEMORY_SCOPE_WORKGROUP) < 4u) { }
      CBAR();
      f32x4 mbv[2], riv[2];
#pragma unroll
      for (int i = 0; i < 2; ++i) {
        mbv[i] = *(const f32x4*)(stat_mb + wsn * 32 + 8 * q + 4 * (i ^ weh));
        riv[i] = *(const f32x4*)(stat_ri + wsn * 32 + 8 * q + 4 * (i ^ weh));
      }
#pragma unroll
      for (int jp = 0; jp < 4; ++jp) {
        const int j = jp ^ (2 * wsn);
        const int e6 = j * 16 + c;
        const int ii = ((e6 >> 5) << 1) | ((e6 >> 2) & 1), qq = (e6 >> 3) & 3, rr = e6 & 3;
        const int p = weh * 64 + 16 * ii + 4 * qq + rr;
        char* dst = (char*)abt + (size_t)((Qd * 4 + nt1) * 16 + ks1) * 16384 + p * 128;
        const int sw = (p >> 1) & 7;
        const float wsj = stat_ws[weh * 64 + j * 16 + c];
        f32x4 v0 = (acc[0][jp] - mbv[0] * wsj) * riv[0];
        f32x4 v1 = (acc[1][jp] - mbv[1] * wsj) * riv[1];
        const u32 a0 = pack_bf16(v0[0], v0[1]), a1 = pack_bf16(v0[2], v0[3]);
        const u32 b0 = pack_bf16(v1[0], v1[1]), b1 = pack_bf16(v1[2], v1[3]);
        u32x4 o0;
        o0[0] = weh ? b0 : a0; o0[1] = weh ? b1 : a1; o0[2] = weh ? a0 : b0; o0[3] = weh ? a1 : b1;
        st16_dev(dst + (((wsn * 4 + q) ^ sw) * 16), o0);
      }
      VM_DRAIN();
      if (l == 0) {
        const u32 n = __hip_atomic_fetch_add(misc + 2, 1u, __ATOMIC_RELAXED, __HIP_MEMORY_SCOPE_WORKGROUP);
        if (n == 3u) {
          st4_dev(ctl + CTL_FLAG1 + (size_t)((Qd * 4 + nt1) * 16 + ks1) * 128, ctag);
          __hip_atomic_store(misc + 4, 1u, __ATOMIC_RELAXED, __HIP_MEMORY_SCOPE_WORKGROUP);
        }
      }
      STAMP(0,3);
    }
    {
      const int we = w & 1, wt = w >> 1;
      f32x4 acc[4][4] = {};
      const int sw = c >> 1;
      const int ro0 = c * 128 + ((q ^ sw) * 16);
      const int ro1 = c * 128 + (((q + 4) ^ sw) * 16);
      const int aoff = 16384 + we * 8192, boff = wt * 8192;
      bf16x8 a0[4], b0[4], a1[4], b1[4];
      auto rd = [&](bf16x8* af, bf16x8* bf, int buf, int ro) {
        const char* lb = smem + buf * P2_STG + ro;
#pragma unroll
        for (int i = 0; i < 4; ++i) { af[i] = *(const bf16x8*)(lb + aoff + i * 2048); bf[i] = *(const bf16x8*)(lb + boff + i * 2048); }
      };
      auto mm = [&](const bf16x8* af, const bf16x8* bf) {
#pragma unroll
        for (int i = 0; i < 4; ++i)
#pragma unroll
          for (int j = 0; j < 4; ++j)
            acc[i][j] = __builtin_amdgcn_mfma_f32_16x16x32_bf16(af[i], bf[j], acc[i][j], 0, 0, 0);
      };
      LDS_BARRIER();
      STAMP(0,4);
      int buf = 0;
      rd(a0, b0, 0, ro0);
#pragma unroll 1
      for (int k = 0; k < 15; ++k) {
        rd(a1, b1, buf, ro1);
        mm(a0, b0);
        IL();
        SB();
        LDS_BARRIER();
        buf = (buf == 2) ? 0 : buf + 1;
        rd(a0, b0, buf, ro0);
        mm(a1, b1);
        IL();
        SB();
      }
      rd(a1, b1, buf, ro1);
      mm(a0, b0);
      IL();
      SB();
      mm(a1, b1);
      STAMP(0,5);
#pragma unroll
      for (int j = 0; j < 4; ++j) {
        const int tr = wt * 64 + j * 16 + c;
        u32x4 o0, o1;
        o0[0] = pack_bf16(acc[0][j][0], acc[0][j][1]); o0[1] = pack_bf16(acc[0][j][2], acc[0][j][3]);
        o0[2] = pack_bf16(acc[1][j][0], acc[1][j][1]); o0[3] = pack_bf16(acc[1][j][2], acc[1][j][3]);
        o1[0] = pack_bf16(acc[2][j][0], acc[2][j][1]); o1[1] = pack_bf16(acc[2][j][2], acc[2][j][3]);
        o1[2] = pack_bf16(acc[3][j][0], acc[3][j][1]); o1[3] = pack_bf16(acc[3][j][2], acc[3][j][3]);
        *(u32x4*)(smem + P2_STG + tr * 256 + (((we * 8 + q) ^ c) * 16)) = o0;
        *(u32x4*)(smem + P2_STG + tr * 256 + (((we * 8 + 4 + q) ^ c) * 16)) = o1;
      }
    }
  }
  {
    LDS_BARRIER();
#pragma unroll
    for (int i = 0; i < 4; ++i) {
      const int tr = (w * 4 + i) * 4 + q;
      const u32x4 v = *(const u32x4*)(smem + P2_STG + tr * 256 + ((c ^ (tr & 15)) * 16));
      st16_dev(PQ + (size_t)Qd * PS + (size_t)(mtile * 128 + tr) * D_DIM + nt2 * 128 + c * 8, v);
    }
    VM_DRAIN();
    if (l == 0) {
      const u32 n = __hip_atomic_fetch_add(misc + 3, 1u, __ATOMIC_RELAXED, __HIP_MEMORY_SCOPE_WORKGROUP);
      if (n == 7u) {
        st4_dev(ctl + CTL_FLAG2 + (size_t)(mtile * 16 + Qd * 4 + nt2) * 128, misc[0]);
        __hip_atomic_store(misc + 6, 1u, __ATOMIC_RELAXED, __HIP_MEMORY_SCOPE_WORKGROUP);
      }
    }
    STAMP(0,6);
  }

  const u32 tag = misc[0];
  const u32x4 wv = *(const u32x4*)(wr + l * 8);
  {
    const int row = xcd * 256 + slot * 8 + w;
    const size_t roff = (size_t)row * D_DIM + l * 8;
    const u32x4 xv = *(const u32x4*)(x + roff);
    if (w == 4) {
      while (__hip_atomic_load(misc + 6, __ATOMIC_RELAXED, __HIP_MEMORY_SCOPE_WORKGROUP) == 0u) __builtin_amdgcn_s_sleep(1);
      poll_words((const u32*)(ctl + CTL_FLAG2 + (size_t)((row >> 7) * 16 + c) * 128), tag);
    }
    wait_vm_bar<0>();
    STAMP(1,0);
    u32x4 h, hn;
    row_h_hn(PQ + roff, xv, h, hn);
    STAMP(1,1);
    float dot = 0.f;
#pragma unroll
    for (int j = 0; j < 4; ++j) dot = dot2bf(hn[j], wv[j], dot);
    dot = wave_sum(dot);
    if (l == 0) {
      const u32 u = pack_bf16(dot, 0.f) & 0xffffu;
      misc[8 + w] = (u & 0x8000u) ? (~u & 0xffffu) : (u | 0x8000u);
    }
    LDS_BARRIER();
    if (tid < 2) {
      u32x4 g;
      g[0] = tag;
      g[1] = misc[8 + tid * 4] | (misc[9 + tid * 4] << 16);
      g[2] = misc[10 + tid * 4] | (misc[11 + tid * 4] << 16);
      g[3] = tag;
      st16_dev(ctl + CTL_KEYS + (size_t)(xcd * 32 + slot) * 32 + tid * 16, g);
    }
    STAMP(1,2);
    u32x4 o;
#pragma unroll
    for (int j = 0; j < 4; ++j) o[j] = pack_bf16(bf_lo(h[j]) + bf_lo(hn[j]), bf_hi(h[j]) + bf_hi(hn[j]));
    st16_dev(out + roff, o);
    VM_DRAIN();
    if (l == 0) {
      const u32 n = __hip_atomic_fetch_add(misc + 7, 1u, __ATOMIC_RELAXED, __HIP_MEMORY_SCOPE_WORKGROUP);
      if (n == 7u) st4_dev(ctl + CTL_FLAG3 + (size_t)(xcd * 32 + slot) * 128, tag);
    }
  }
  const int d0 = (xcd * 4 + (slot - 28)) * 16;
  if (is_p5) {
    const u16* src = wd + (size_t)(w * 224 + (l >> 1)) * D_DIM + d0 + (l & 1) * 8;
#pragma unroll
    for (int m = 0; m < 7; ++m)
      __builtin_amdgcn_global_load_lds(GLB_PTR(src + (size_t)m * 32 * D_DIM), LDS_PTR(smem + LDS_W + w * 7168 + m * 1024), 16, 0, 0);
  } else {
    const int f0 = (xcd * 28 + slot) * 8;
    const u16* src = ((l & 1) ? wu : wg) + (size_t)(w * 64 + (l >> 1)) * F_DIM + f0;
#pragma unroll
    for (int m = 0; m < 2; ++m)
      __builtin_amdgcn_global_load_lds(GLB_PTR(src + (size_t)m * 32 * F_DIM), LDS_PTR(smem + LDS_W + w * 2048 + m * 1024), 16, 0, 0);
  }

  u32* cand = (u32*)smem;
  u32* gmax = cand + 64;
  u32* cnt = cand + 72;
  u32* sel = cand + 80;
  char* red = smem + 4096;
  char* hnsel = smem + 16384;
  char* hsel = smem + 32768;
  u32 selkey;
  {
    if (tid < 64) cand[tid] = 0;
    if (tid == 0) *cnt = 0;
    u32x4 g;
#pragma unroll 1
    for (int spin = 0; spin < SPIN_CAP; ++spin) {
      g = ld16_dev_wait(ctl + CTL_KEYS + tid * 16);
      if (__all(g[0] == tag && g[3] == tag)) break;
      __builtin_amdgcn_s_sleep(2);
    }
    STAMP(1,3);
    const u32 ib = (u32)(S_LEN - 1 - tid * 4);
    u32 a0 = (g[1] << 16) | ib, a1 = (g[1] & 0xffff0000u) | (ib - 1), a2 = (g[2] << 16) | (ib - 2), a3 = (g[2] & 0xffff0000u) | (ib - 3);
    {
      const u32 m = wave_max_u32(umax(umax(a0, a1), umax(a2, a3)));
      if (l == 0) gmax[w] = m;
    }
    LDS_BARRIER();
    {
      const u32x4 g0 = *(const u32x4*)(gmax), g1 = *(const u32x4*)(gmax + 4);
      const u32 T = umin(umin(umin(g0[0], g0[1]), umin(g0[2], g0[3])), umin(umin(g1[0], g1[1]), umin(g1[2], g1[3])));
      const u32 av[4] = {a0, a1, a2, a3};
      unsigned long long mk[4];
      u32 pre[4], tot = 0;
#pragma unroll
      for (int j = 0; j < 4; ++j) { mk[j] = __ballot(av[j] >= T); pre[j] = tot; tot += (u32)__builtin_popcountll(mk[j]); }
      u32 base = 0;
      if (l == 0) base = __hip_atomic_fetch_add(cnt, tot, __ATOMIC_RELAXED, __HIP_MEMORY_SCOPE_WORKGROUP);
      base = __builtin_amdgcn_readfirstlane(base);
#pragma unroll
      for (int j = 0; j < 4; ++j) {
        const u32 pos = base + pre[j] + __builtin_amdgcn_mbcnt_hi((u32)(mk[j] >> 32), __builtin_amdgcn_mbcnt_lo((u32)mk[j], 0u));
        if (av[j] >= T && pos < 64) cand[pos] = av[j];
      }
    }
    LDS_BARRIER();
    const u32 n = __builtin_amdgcn_readfirstlane(*cnt);
    if (n <= 64) {
      const u32 mine = cand[l];
      int rank = 0;
      for (u32 i0 = 0; i0 < n; i0 += 16) {
#pragma unroll
        for (int i = 0; i < 4; ++i) {
          const u32x4 v = *(const u32x4*)(cand + i0 + i * 4);
          rank += (v[0] > mine) + (v[1] > mine) + (v[2] > mine) + (v[3] > mine);
        }
      }
      if (rank < 8 && (u32)l < n) sel[w * 8 + rank] = mine;
      asm volatile("s_waitcnt lgkmcnt(0)" ::: "memory");
      selkey = sel[w * 8 + (l & 7)];
    } else {
      CE(a0, a1) CE(a2, a3) CE(a0, a2) CE(a1, a3) CE(a1, a2)
      u32 mycand = 0;
#pragma unroll 1
      for (int it = 0; it < 8; ++it) {
        const u32 m = wave_max_u32(a0);
        const bool win = (a0 == m);
        a0 = win ? a1 : a0; a1 = win ? a2 : a1; a2 = win ? a3 : a2; a3 = win ? 0u : a3;
        mycand = (l == it) ? m : mycand;
      }
      LDS_BARRIER();
      if (l < 8) cand[w * 8 + l] = mycand;
      LDS_BARRIER();
      const u32 mine = cand[l];
      int rank = 0;
#pragma unroll 1
      for (int i = 0; i < 16; ++i) {
        const u32x4 v = *(const u32x4*)(cand + i * 4);
        rank += (v[0] > mine) + (v[1] > mine) + (v[2] > mine) + (v[3] > mine);
      }
      if (rank < 8) sel[w * 8 + rank] = mine;
      asm volatile("s_waitcnt lgkmcnt(0)" ::: "memory");
      selkey = sel[w * 8 + (l & 7)];
    }
  }
  STAMP(1,4);
  const int myrow = (S_LEN - 1 - (int)(selkey & 0xffffu)) & (S_LEN - 1);
  {
    const int trow = __builtin_amdgcn_readlane(myrow, w);
    const size_t roff = (size_t)trow * D_DIM + l * 8;
    const u32x4 xv = *(const u32x4*)(x + roff);
    u32x4 h, hn;
    row_h_hn(PQ + roff, xv, h, hn);
    *(u32x4*)(hnsel + w * 1040 + l * 16) = hn;
    *(u32x4*)(hsel + w * 1040 + l * 16) = h;
  }
  LDS_BARRIER();
  if (!is_p5) {
    const int ct = xcd * 28 + slot;
    f32x4 acc = {};
    {
      const char* base = smem + LDS_W + w * 2048 + q * 256 + c * 8;
#pragma unroll
      for (int ks = 0; ks < 2; ++ks) {
        const bf16x8 xf = *(const bf16x8*)(hnsel + (c & 7) * 1040 + w * 128 + ks * 64 + q * 16);
        s16x4 lo = __builtin_bit_cast(s16x4, __builtin_amdgcn_ds_read_tr16_b64_v4i16(
                       (__attribute__((address_space(3))) s16x4*)(base + ks * 1024)));
        s16x4 hi = __builtin_bit_cast(s16x4, __builtin_amdgcn_ds_read_tr16_b64_v4i16(
                       (__attribute__((address_space(3))) s16x4*)(base + ks * 1024 + 128)));
        bf16x8 a = {lo[0], lo[1], lo[2], lo[3], hi[0], hi[1], hi[2], hi[3]};
        acc = __builtin_amdgcn_mfma_f32_16x16x32_bf16(a, xf, acc, 0, 0, 0);
      }
    }
    *(f32x4*)(red + w * 1024 + l * 16) = acc;
    LDS_BARRIER();
    if (w == 0) {
      if (l < 32 && c < 8) {
        f32x4 g = *(const f32x4*)(red + l * 16);
        f32x4 u = *(const f32x4*)(red + (l + 32) * 16);
#pragma unroll
        for (int ww = 1; ww < 8; ++ww) {
          g += *(const f32x4*)(red + ww * 1024 + l * 16);
          u += *(const f32x4*)(red + ww * 1024 + (l + 32) * 16);
        }
        float o[4];
#pragma unroll
        for (int r = 0; r < 4; ++r) {
          const float gp = rbf(g[r]);
          const float sg = rbf(gp / (1.0f + __expf(-gp)));
          o[r] = sg * rbf(u[r]);
        }
        u32x4 ov = {tag, pack_bf16(o[0], o[1]), pack_bf16(o[2], o[3]), tag};
        st16_dev(ctl + CTL_GU + (size_t)ct * 256 + (q * 8 + c) * 16, ov);
      }
    }
    STAMP(1,5);
    return;
  }

  {
    if (w == 0) poll_words((const u32*)(ctl + CTL_FLAG3 + (size_t)(myrow >> 3) * 128), tag);
    bf16x8 gf[7];
    {
      const char* src = ctl + CTL_GU + (size_t)(w * 28 + q) * 256 + c * 16;
#pragma unroll 1
      for (int spin = 0; spin < SPIN_CAP; ++spin) {
        const u32x4 g = ld16_dev_wait(src);
        if (__all(g[0] == tag && g[3] == tag)) break;
        __builtin_amdgcn_s_sleep(2);
      }
#pragma unroll 1
      for (int spin = 0; spin < SPIN_CAP; ++spin) {
        u32x4 g[7];
        asm volatile(
            "global_load_dwordx4 %0, %7, off sc1\n\tglobal_load_dwordx4 %1, %7, off offset:1024 sc1\n\t"
            "global_load_dwordx4 %2, %7, off offset:2048 sc1\n\tglobal_load_dwordx4 %3, %7, off offset:3072 sc1\n\t"
            "global_load_dwordx4 %4, %8, off sc1\n\tglobal_load_dwordx4 %5, %8, off offset:1024 sc1\n\t"
            "global_load_dwordx4 %6, %8, off offset:2048 sc1\n\ts_waitcnt vmcnt(0)"
            : "=&v"(g[0]), "=&v"(g[1]), "=&v"(g[2]), "=&v"(g[3]), "=&v"(g[4]), "=&v"(g[5]), "=&v"(g[6])
            : "v"(src), "v"(src + 4096) : "memory");
        bool ok = true;
#pragma unroll
        for (int i = 0; i < 7; ++i) ok = ok && (g[i][0] == tag) && (g[i][3] == tag);
#pragma unroll
        for (int ks = 0; ks < 7; ++ks) {
          const u32x4 f = {g[ks][1], g[ks][2], dppu<0x128>(g[ks][1]), dppu<0x128>(g[ks][2])};
          gf[ks] = __builtin_bit_cast(bf16x8, f);
        }
        if (__all(ok)) break;
        __builtin_amdgcn_s_sleep(1);
      }
    }
    STAMP(1,5);
    f32x4 acc = {};
    {
      const char* base = smem + LDS_W + w * 7168 + q * 256 + c * 8;
#pragma unroll
      for (int ks = 0; ks < 7; ++ks) {
        s16x4 lo = __builtin_bit_cast(s16x4, __builtin_amdgcn_ds_read_tr16_b64_v4i16(
                       (__attribute__((address_space(3))) s16x4*)(base + ks * 1024)));
        s16x4 hi = __builtin_bit_cast(s16x4, __builtin_amdgcn_ds_read_tr16_b64_v4i16(
                       (__attribute__((address_space(3))) s16x4*)(base + ks * 1024 + 128)));
        bf16x8 a = {lo[0], lo[1], lo[2], lo[3], hi[0], hi[1], hi[2], hi[3]};
        acc = __builtin_amdgcn_mfma_f32_16x16x32_bf16(a, gf[ks], acc, 0, 0, 0);
      }
    }
    *(f32x4*)(red + w * 1024 + l * 16) = acc;
    LDS_BARRIER();
    if (b == 255 && tid == 64) st4_dev(ctl + CTL_EPOCH, tag);
    if (w == 0 && c < 8) {
      f32x4 y = *(const f32x4*)(red + l * 16);
#pragma unroll
      for (int ww = 1; ww < 8; ++ww) y += *(const f32x4*)(red + ww * 1024 + l * 16);
      const u32 key = selkey >> 16;
      const u32 u = (key & 0x8000u) ? (key & 0x7fffu) : (~key & 0xffffu);
      const float sg = rbf(1.0f / (1.0f + __expf(-__uint_as_float(u << 16))));
      const u32x2 hnv = *(const u32x2*)(hnsel + c * 1040 + (d0 + q * 4) * 2);
      const u32x2 hh = *(const u32x2*)(hsel + c * 1040 + (d0 + q * 4) * 2);
      float hn4[4] = {bf_lo(hnv[0]), bf_hi(hnv[0]), bf_lo(hnv[1]), bf_hi(hnv[1])};
      float h4[4] = {bf_lo(hh[0]), bf_hi(hh[0]), bf_lo(hh[1]), bf_hi(hh[1])};
      float o[4];
#pragma unroll
      for (int r = 0; r < 4; ++r) {
        const float yy = rbf(rbf(y[r]) * sg);
        o[r] = h4[r] + rbf(hn4[r] + yy);
      }
      u32x2 ov = {pack_bf16(o[0], o[1]), pack_bf16(o[2], o[3])};
      st8_dev(out + (size_t)myrow * D_DIM + d0 + q * 4, ov);
    }
    STAMP(1,6);
    asm volatile("fnet_code_end:");
  }
}

extern "C" void kernel_launch(void* const* d_in, const int* in_sizes, int n_in, void* d_out, int out_size, void* d_ws,
                              size_t ws_size, hipStream_t stream) {
  fnet_moe_kernel<<<256, 512, 0, stream>>>((const u16*)d_in[0], (const u16*)d_in[1], (const u16*)d_in[2], (const u16*)d_in[3],
                                           (const u16*)d_in[4], (const u16*)d_in[6], (const u16*)d_in[7], (const u16*)d_in[8],
                                           (const u16*)d_in[9], (u16*)d_out, (char*)d_ws);
}
```

```cpp
#include <hip/hip_runtime.h>
#include <hip/hip_bf16.h>
#include <stdint.h>

typedef unsigned short u16;
typedef unsigned int u32;
typedef __attribute__((ext_vector_type(8))) short bf16x8;
typedef __attribute__((ext_vector_type(4))) short s16x4;
typedef __attribute__((ext_vector_type(4))) float f32x4;
typedef __attribute__((ext_vector_type(2))) float f32x2;
typedef __attribute__((ext_vector_type(2))) __bf16 bf16x2_t;
typedef __attribute__((ext_vector_type(4))) u32 u32x4;
typedef __attribute__((ext_vector_type(2))) u32 u32x2;


#define S_LEN 2048
#define D_DIM 512
#define F_DIM 1792

#define LDS_PTR(p) ((__attribute__((address_space(3))) void*)(p))
#define GLB_PTR(p) ((const __attribute__((address_space(1))) void*)(p))
#define SB() __builtin_amdgcn_sched_barrier(0)
#define CBAR() asm volatile("" ::: "memory")
#define IL1() __builtin_amdgcn_sched_group_barrier(0x008, 1, 0); __builtin_amdgcn_sched_group_barrier(0x100, 1, 0);
#define IL() do { IL1() IL1() IL1() IL1() IL1() IL1() IL1() IL1() __builtin_amdgcn_sched_group_barrier(0x008, 8, 0); } while (0)
#define LDS_BARRIER() asm volatile("s_waitcnt lgkmcnt(0)\n\ts_barrier" ::: "memory")

__device__ __forceinline__ u32 pack_bf16(float lo, float hi) {
  f32x2 v = {lo, hi};
  bf16x2_t b = __builtin_convertvector(v, bf16x2_t);
  return __builtin_bit_cast(u32, b);
}
__device__ __forceinline__ float bf_lo(u32 u) { return __uint_as_float(u << 16); }
__device__ __forceinline__ float bf_hi(u32 u) { return __uint_as_float(u & 0xffff0000u); }
__device__ __forceinline__ float rbf(float x) { return bf_lo(pack_bf16(x, 0.f)); }
__device__ __forceinline__ float dot2bf(u32 a, u32 b, float acc) {
  return __builtin_amdgcn_fdot2_f32_bf16(__builtin_bit_cast(bf16x2_t, a), __builtin_bit_cast(bf16x2_t, b), acc, false);
}

template <int CTRL>
__device__ __forceinline__ float dppf(float x) {
  return __builtin_bit_cast(float, __builtin_amdgcn_mov_dpp(__builtin_bit_cast(int, x), CTRL, 0xf, 0xf, true));
}
template <int CTRL>
__device__ __forceinline__ u32 dppu(u32 x) {
  return (u32)__builtin_amdgcn_mov_dpp((int)x, CTRL, 0xf, 0xf, true);
}
__device__ __forceinline__ float xrow_sum(float x) {
  auto s = __builtin_amdgcn_permlane16_swap(__float_as_uint(x), __float_as_uint(x), false, false);
  x = __uint_as_float(s[0]) + __uint_as_float(s[1]);
  auto t = __builtin_amdgcn_permlane32_swap(__float_as_uint(x), __float_as_uint(x), false, false);
  return __uint_as_float(t[0]) + __uint_as_float(t[1]);
}
__device__ __forceinline__ float wave_sum(float x) {
  x += dppf<0xB1>(x);
  x += dppf<0x4E>(x);
  x += dppf<0x141>(x);
  x += dppf<0x140>(x);
  return xrow_sum(x);
}
__device__ __forceinline__ u32 umax(u32 a, u32 b) { return a > b ? a : b; }
__device__ __forceinline__ u32 umin(u32 a, u32 b) { return a < b ? a : b; }
__device__ __forceinline__ u32 wave_max_u32(u32 x) {
  x = umax(x, dppu<0xB1>(x));
  x = umax(x, dppu<0x4E>(x));
  x = umax(x, dppu<0x141>(x));
  x = umax(x, dppu<0x140>(x));
  auto s = __builtin_amdgcn_permlane16_swap(x, x, false, false);
  x = umax(s[0], s[1]);
  auto t = __builtin_amdgcn_permlane32_swap(x, x, false, false);
  x = umax(t[0], t[1]);
  return x;
}
__device__ __forceinline__ void ln_scalars(float s1, float s2, float& mb, float& rinv) {
  const float mean = s1 * (1.0f / D_DIM);
  const float var = fmaxf(s2 * (1.0f / D_DIM) - mean * mean, 0.f);
  mb = rbf(mean);
  const float vb = rbf(var);
  const float sd = rbf(__builtin_amdgcn_sqrtf(rbf(vb + 1e-5f)));
  rinv = __builtin_amdgcn_rcpf(sd);
}


template <int N> __device__ __forceinline__ void wait_vm_bar() { asm volatile("s_waitcnt vmcnt(%0) lgkmcnt(0)\n\ts_barrier" ::"n"(N) : "memory"); }
#define VM_DRAIN() asm volatile("s_waitcnt vmcnt(0)" ::: "memory")

__device__ __forceinline__ void st16_dev(void* p, u32x4 v) {
  asm volatile("global_store_dwordx4 %0, %1, off sc1\n\ts_nop 1" ::"v"(p), "v"(v) : "memory");
}
__device__ __forceinline__ void st8_dev(void* p, u32x2 v) {
  asm volatile("global_store_dwordx2 %0, %1, off sc1\n\ts_nop 1" ::"v"(p), "v"(v) : "memory");
}
__device__ __forceinline__ void st4_dev(void* p, u32 v) {
  asm volatile("global_store_dword %0, %1, off sc1" ::"v"(p), "v"(v) : "memory");
}
__device__ __forceinline__ u32 ld4_dev_wait(const void* p) {
  u32 v;
  asm volatile("global_load_dword %0, %1, off sc1\n\ts_waitcnt vmcnt(0)" : "=&v"(v) : "v"(p) : "memory");
  return v;
}
__device__ __forceinline__ u32x4 ld16_dev_wait(const void* p) {
  u32x4 v;
  asm volatile("global_load_dwordx4 %0, %1, off sc1\n\ts_waitcnt vmcnt(0)" : "=&v"(v) : "v"(p) : "memory");
  return v;
}
#define SPIN_CAP 20000
__device__ __forceinline__ void poll_words(const u32* p, u32 tag) {
#pragma unroll 1
  for (int spin = 0; spin < SPIN_CAP; ++spin) {
    const u32 v = ld4_dev_wait(p);
    if (__all(v == tag)) break;
    __builtin_amdgcn_s_sleep(1);
  }
}

__device__ __forceinline__ u32x4 compute_h8(const u32x4& p0, const u32x4& p1, const u32x4& q0, const u32x4& q1, const u32x4& xv) {
  u32x4 h;
#pragma unroll
  for (int j = 0; j < 4; ++j) {
    u32 p = pack_bf16(bf_lo(p0[j]) + bf_lo(p1[j]), bf_hi(p0[j]) + bf_hi(p1[j]));
    u32 qq = pack_bf16(bf_lo(q0[j]) + bf_lo(q1[j]), bf_hi(q0[j]) + bf_hi(q1[j]));
    u32 y = pack_bf16(bf_lo(p) - bf_lo(qq), bf_hi(p) - bf_hi(qq));
    h[j] = pack_bf16(bf_lo(xv[j]) + bf_lo(y), bf_hi(xv[j]) + bf_hi(y));
  }
  return h;
}
__device__ __forceinline__ void row_h_hn(const u16* pq, const u32x4& xv, u32x4& h, u32x4& hn) {
  const size_t PS = (size_t)S_LEN * D_DIM;
  u32x4 p0, p1, q0, q1;
  asm volatile(
      "global_load_dwordx4 %0, %4, off sc1\n\tglobal_load_dwordx4 %1, %5, off sc1\n\t"
      "global_load_dwordx4 %2, %6, off sc1\n\tglobal_load_dwordx4 %3, %7, off sc1\n\ts_waitcnt vmcnt(0)"
      : "=&v"(p0), "=&v"(p1), "=&v"(q0), "=&v"(q1)
      : "v"(pq), "v"(pq + PS), "v"(pq + 2 * PS), "v"(pq + 3 * PS)
      : "memory");
  h = compute_h8(p0, p1, q0, q1, xv);
  float s1 = 0.f, s2 = 0.f;
#pragma unroll
  for (int j = 0; j < 4; ++j) { s1 = dot2bf(h[j], 0x3f803f80u, s1); s2 = dot2bf(h[j], h[j], s2); }
  s1 = wave_sum(s1);
  s2 = wave_sum(s2);
  float mb, rinv;
  ln_scalars(s1, s2, mb, rinv);
#pragma unroll
  for (int j = 0; j < 4; ++j) {
    u32 dd = pack_bf16(bf_lo(h[j]) - mb, bf_hi(h[j]) - mb);
    hn[j] = pack_bf16(bf_lo(dd) * rinv, bf_hi(dd) * rinv);
  }
}
#define CE(a, b) { u32 _hi = umax(a, b), _lo = umin(a, b); a = _hi; b = _lo; }

#define WS_ABT 0
#define WS_PQ (4u << 20)
#define WS_CTL (14u << 20)
#define CTL_EPOCH 0
#define CTL_KEYS 16384
#define CTL_FLAG1 65536
#define CTL_FLAG2 131072
#define CTL_FLAG3 196608
#define CTL_GU 262144
#define TAG_INC 0x9E3779B9u

#define P1_W 65536
#define P1_NST 5
#define P2_STG 32768
#define LDS_W 98304
#define LDS_MISC 155648
#define LDS_TOTAL 163840

__global__ __launch_bounds__(512, 2) void fnet_moe_kernel(const u16* x, const u16* Wsr, const u16* Wsi, const u16* Dr,
                                                          const u16* Di, const u16* wr, const u16* wg, const u16* wu,
                                                          const u16* wd, u16* out, char* ws) {
  __shared__ __attribute__((aligned(1024))) char smem[LDS_TOTAL];
  float* stat_mb = (float*)(smem + LDS_MISC);
  float* stat_ri = stat_mb + 64;
  float* stat_ws = stat_mb + 128;
  u32* misc = (u32*)(smem + LDS_MISC + 1024);
  const int b = blockIdx.x, xcd = b & 7, slot = b >> 3;
  const int Qd = xcd & 3, part = Qd >> 1, sh = Qd & 1;
  const int tid = threadIdx.x, w = __builtin_amdgcn_readfirstlane(tid >> 6), l = tid & 63;
  const int c = l & 15, q = l >> 4;
  char* ctl = ws + WS_CTL;
  u16* abt = (u16*)(ws + WS_ABT);
  u16* PQ = (u16*)(ws + WS_PQ);
  const size_t PS = (size_t)S_LEN * D_DIM;
  const int nt1 = (xcd >> 2) * 2 + (slot >> 4), ks1 = slot & 15;
  const int part1 = part, Qd1 = Qd, st1 = sh * 16 + ks1;
  const int mt = slot >> 2, nt2 = slot & 3, mtile = (xcd >> 2) * 8 + mt;
  const bool is_p5 = slot >= 28;
  if (w >= 4) {
    const int lw = w - 4, lt = tid - 256;
    const u16* Wp = part1 ? Di : Dr;
    const u16* Wsx = part ? Wsi : Wsr;
    u32 pf1 = 0, pf2 = 0, pf3, pf4;
    const char* pA = (const char*)(Wsx + (size_t)((xcd >> 2) * 1024 + slot * 32 + lw * 8 + ((l >> 2) & 7)) * S_LEN + sh * 1024) +
                     ((l & 3) + 4 * (l >> 5)) * 128;
    u32 pf5 = 0;
    {
      const int s0 = st1 * 64;
      if (lw == 0) {
        const char* p = (const char*)(Wp + (size_t)(xcd >> 2) * 256 * D_DIM) + (size_t)slot * 8192 + l * 128;
        asm volatile("global_load_dword %0, %1, off" : "=v"(pf1) : "v"(p) : "memory");
      }
      const char* xsrc[2];
#pragma unroll
      for (int jj = 0; jj < 2; ++jj) {
        const int p = (lw * 2 + jj) * 8 + (l >> 3);
        const int srow = 32 * (p >> 5) + 8 * ((p >> 2) & 3) + 4 * ((p >> 4) & 1) + (p & 3);
        xsrc[jj] = (const char*)(x + (size_t)(s0 + srow) * D_DIM) + (((l & 7) ^ ((p >> 1) & 7)) * 16);
      }
      const char* wsrc0 = (const char*)(Wp + (size_t)(nt1 * 128 + lw * 32 + (l >> 3)) * D_DIM) + (((l & 7) ^ (l >> 4)) * 16);
      const char* wsrc1 = (const char*)(Wp + (size_t)(nt1 * 128 + lw * 32 + 8 + (l >> 3)) * D_DIM) + (((l & 7) ^ (4 + (l >> 4))) * 16);
      auto issueX = [&](int kq) {
#pragma unroll
        for (int jj = 0; jj < 2; ++jj)
          __builtin_amdgcn_global_load_lds(GLB_PTR(xsrc[jj] + kq * 128), LDS_PTR(smem + kq * 8192 + (lw * 2 + jj) * 1024), 16, 0, 0);
      };
      auto issueW = [&](int kq) {
        char* lb = smem + P1_W + (kq % P1_NST) * 16384;
#pragma unroll
        for (int jj = 0; jj < 4; ++jj)
          __builtin_amdgcn_global_load_lds(GLB_PTR(((jj & 1) ? wsrc1 : wsrc0) + (jj >> 1) * 16384 + kq * 128),
                                           LDS_PTR(lb + (lw * 4 + jj) * 1024), 16, 0, 0);
      };
      issueX(0); issueX(1); issueX(2); issueX(3);
      issueW(0);
      issueW(1);
      wait_vm_bar<4>();
      issueX(4); issueX(5); issueX(6); issueX(7);
      wait_vm_bar<8>();
      issueW(2);
      issueW(3);
      wait_vm_bar<4>();
      issueW(4);
      issueW(5);
      if (l < 32) asm volatile("global_load_dword %0, %1, off" : "=v"(pf2) : "v"(pA) : "memory");
      wait_vm_bar<9>();
      issueW(6);
      wait_vm_bar<9>();
      issueW(7);
      wait_vm_bar<9>();
      if (l >= 32) asm volatile("global_load_dword %0, %1, off" : "=v"(pf5) : "v"(pA) : "memory");
      wait_vm_bar<5>();
      wait_vm_bar<1>();
    }
    {
      const int t0 = mtile * 128;
      const char* Abase = (const char*)(Wsx + (size_t)t0 * S_LEN + sh * 1024);
      const char* Bbase = (const char*)abt + (size_t)(Qd * 4 + nt2) * 16 * 16384;
      const u32 av0 = (u32)(l >> 3) * 4096u + (u32)((l & 7) ^ (l >> 4)) * 16u;
      const u32 av1 = (u32)(l >> 3) * 4096u + (u32)((l & 7) ^ (4 + (l >> 4))) * 16u;
      const u32 bv = (u32)l * 16u;
      const int k0 = mt + 2 * nt2;
      auto kst = [&](int k) { return ((k + k0) & 7) | (k & 8); };
      auto issueA = [&](int k, int buf) {
        const char* ab = Abase + kst(k) * 128;
        char* lb = smem + buf * P2_STG;
#pragma unroll
        for (int jj = 0; jj < 4; ++jj) {
          const int ja = lw * 4 + jj;
          __builtin_amdgcn_global_load_lds(GLB_PTR(ab + (size_t)ja * 32768 + ((jj & 1) ? av1 : av0)), LDS_PTR(lb + ja * 1024), 16, 0, 0);
        }
      };
      auto issueB = [&](int k, int buf) {
        const char* bb = Bbase + (size_t)kst(k) * 16384;
        char* lb = smem + buf * P2_STG;
#pragma unroll
        for (int jj = 0; jj < 4; ++jj) {
          const int ja = lw * 4 + jj;
          __builtin_amdgcn_global_load_lds(GLB_PTR(bb + ja * 1024 + bv), LDS_PTR(lb + 16384 + ja * 1024), 16, 0, 16);
        }
      };
      while (__hip_atomic_load(misc + 4, __ATOMIC_RELAXED, __HIP_MEMORY_SCOPE_WORKGROUP) == 0u) __builtin_amdgcn_s_sleep(1);
      CBAR();
      const u32 tag = misc[0];
      issueA(0, 0);
      issueA(1, 1);
      issueA(2, 2);

      if (lw == 0) {
        poll_words((const u32*)(ctl + CTL_FLAG1 + (size_t)((Qd * 4 + nt2) * 16 + c) * 128), tag);
        if (l == 0) __hip_atomic_store(misc + 5, 1u, __ATOMIC_RELAXED, __HIP_MEMORY_SCOPE_WORKGROUP);
      } else {
        while (__hip_atomic_load(misc + 5, __ATOMIC_RELAXED, __HIP_MEMORY_SCOPE_WORKGROUP) == 0u) __builtin_amdgcn_s_sleep(1);
      }
      CBAR();
      issueB(0, 0);
      issueB(1, 1);
      wait_vm_bar<4>();
      issueB(2, 2);
      wait_vm_bar<4>();
      issueA(3, 0); issueB(3, 0);
      asm volatile("global_load_dword %0, %1, off" : "=v"(pf3) : "v"(pA + 1024) : "memory");
      wait_vm_bar<9>();
      issueA(4, 1); issueB(4, 1);
      wait_vm_bar<9>();
      issueA(5, 2); issueB(5, 2);
      int nbuf = 0;
#pragma unroll 1
      for (int k = 4; k < 10; ++k) {
        wait_vm_bar<8>();
        issueA(k + 2, nbuf); issueB(k + 2, nbuf);
        nbuf = (nbuf == 2) ? 0 : nbuf + 1;
      }
      {
        const char* p;
        if (lt < 128) {
          const int r = slot * 16 + (lt >> 3), mat = (lt >> 2) & 1, ln = lt & 3;
          p = (const char*)((mat ? wu : wg) + (size_t)r * F_DIM) + ((xcd * 448) & ~127) + ln * 128;
        } else {
          int r = lt - 128;
          if (r > 55) r = 55;
          p = (const char*)(wd + (size_t)(slot * 56 + r) * D_DIM) + xcd * 128;
        }
        asm volatile("global_load_dword %0, %1, off" : "=v"(pf4) : "v"(p) : "memory");
      }
      wait_vm_bar<9>();
      issueA(12, 0); issueB(12, 0);
      wait_vm_bar<9>();
      issueA(13, 1); issueB(13, 1);
      wait_vm_bar<8>();
      issueA(14, 2); issueB(14, 2);
      wait_vm_bar<8>();
      issueA(15, 0); issueB(15, 0);
      wait_vm_bar<8>();
      wait_vm_bar<0>();
      asm volatile("" ::"v"(pf1), "v"(pf2), "v"(pf3), "v"(pf4), "v"(pf5));
    }
  } else {
    __builtin_amdgcn_s_setprio(2);
    u32 cpf = 0;
    if (w == 1) {
      unsigned long long pc;
      u32 len;
      asm volatile("s_getpc_b64 %0\nfnet_code_begin:\n\ts_mov_b32 %1, fnet_code_end-fnet_code_begin" : "=s"(pc), "=s"(len));
      const u32 off = (u32)(slot + 32 * l) * 128u;
      if (off < len) asm volatile("global_load_dword %0, %1, off" : "=v"(cpf) : "v"((const char*)pc + off) : "memory");
    }
    u32 epoch;
    asm volatile("global_load_dword %0, %1, off sc1" : "=v"(epoch) : "v"(ctl + CTL_EPOCH) : "memory");
    if (tid < 8) misc[tid] = 0;
    {
      const int wsn = w & 1, weh = w >> 1;
      f32x4 acc[2][4] = {};
      f32x4 sx = {}, sq = {}, sw0 = {}, sw1v = {};
      const bf16x8 ones = {0x3f80, 0x3f80, 0x3f80, 0x3f80, 0x3f80, 0x3f80, 0x3f80, 0x3f80};
      const int sw1 = c >> 1;
      const int ro0 = c * 128 + ((q ^ sw1) * 16), ro1 = c * 128 + (((q + 4) ^ sw1) * 16);
      const int aoff = wsn * 4096 + weh * 2048;
      const int boff = P1_W + weh * 8192 + wsn * 4096;
      bf16x8 af[2][2], bfr[2][4];
      auto rd = [&](int st, int kq, int slotw) {
        const char* la = smem + kq * 8192 + (st ? ro1 : ro0);
        const char* lb = smem + slotw * 16384 + (st ? ro1 : ro0);
#pragma unroll
        for (int i = 0; i < 2; ++i) af[st][i] = *(const bf16x8*)(la + (aoff ^ (i * 2048)));
#pragma unroll
        for (int j = 0; j < 4; ++j) bfr[st][j] = *(const bf16x8*)(lb + (boff ^ ((j >> 1) * 4096)) + (j & 1) * 2048);
      };
      auto mm = [&](int st) {
        sx = __builtin_amdgcn_mfma_f32_16x16x32_bf16(af[st][0], ones, sx, 0, 0, 0);
        sq = __builtin_amdgcn_mfma_f32_16x16x32_bf16(af[st][0], af[st][0], sq, 0, 0, 0);
        sw0 = __builtin_amdgcn_mfma_f32_16x16x32_bf16(ones, bfr[st][0], sw0, 0, 0, 0);
        sw1v = __builtin_amdgcn_mfma_f32_16x16x32_bf16(ones, bfr[st][1], sw1v, 0, 0, 0);
#pragma unroll
        for (int i = 0; i < 2; ++i)
#pragma unroll
          for (int j = 0; j < 4; ++j)
            acc[i][j] = __builtin_amdgcn_mfma_f32_16x16x32_bf16(af[st][i], bfr[st][j], acc[i][j], 0, 0, 0);
      };
#define IL6() do { IL1() IL1() IL1() IL1() IL1() IL1() __builtin_amdgcn_sched_group_barrier(0x008, 6, 0); } while (0)
      LDS_BARRIER();
      if (w == 2 && l < 12) {
        const size_t prow = (size_t)(xcd * 256 + slot * 8) * D_DIM;
        const char* p = l < 4   ? (const char*)(PQ + (size_t)l * PS + prow)
                        : l == 4 ? (const char*)(PQ + (size_t)Qd * PS + (size_t)(mtile * 128) * D_DIM)
                        : l == 5 ? (const char*)(out + prow)
                        : l == 6 ? (const char*)abt + (size_t)((Qd1 * 4 + nt1) * 16 + ks1) * 16384
                        : l == 7 ? (const char*)abt + (size_t)(Qd * 4 + nt2) * 16 * 16384
                        : l == 8 ? (const char*)wg
                        : l == 9 ? (const char*)wu
                        : l == 10 ? (const char*)wd : (const char*)wr;
        asm volatile("global_load_dword %0, %1, off sc1" : "=v"(cpf) : "v"(p) : "memory");
      }
      rd(0, 0, 0);
      int stg = 0;
#pragma unroll 1
      for (int k = 0; k < 7; ++k) {
        rd(1, k, stg); mm(0); IL6(); SB();
        LDS_BARRIER();
        stg = (stg + 1 == P1_NST) ? 0 : stg + 1;
        rd(0, k + 1, stg); mm(1); IL6(); SB();
      }
      rd(1, 7, stg); mm(0); IL6(); SB();
      mm(1);
      asm volatile("s_waitcnt vmcnt(0)" : "+v"(epoch), "+v"(cpf)::"memory");
      const u32 ctag = epoch + TAG_INC;
      if (tid == 0) misc[0] = ctag;
      {
        const int r = c & 3;
        const float s1 = r == 0 ? sx[0] : r == 1 ? sx[1] : r == 2 ? sx[2] : sx[3];
        const float s2 = r == 0 ? sq[0] : r == 1 ? sq[1] : r == 2 ? sq[2] : sq[3];
        float mb, ri;
        ln_scalars(s1, s2, mb, ri);
        if (q == (c >> 2)) {
          const int srow = wsn * 32 + 8 * (c >> 2) + 4 * weh + (c & 3);
          stat_mb[srow] = mb; stat_ri[srow] = ri;
        }
        if (q == 0) {
          stat_ws[weh * 64 + (2 * wsn) * 16 + c] = sw0[0];
          stat_ws[weh * 64 + (2 * wsn + 1) * 16 + c] = sw1v[0];
        }
      }
      asm volatile("s_waitcnt lgkmcnt(0)" ::: "memory");
      if (l == 0) __hip_atomic_fetch_add(misc + 1, 1u, __ATOMIC_RELAXED, __HIP_MEMORY_SCOPE_WORKGROUP);
      while (__hip_atomic_load(misc + 1, __ATOMIC_RELAXED, __HIP_MEMORY_SCOPE_WORKGROUP) < 4u) { }
      CBAR();
      f32x4 mbv[2], riv[2];
#pragma unroll
      for (int i = 0; i < 2; ++i) {
        mbv[i] = *(const f32x4*)(stat_mb + wsn * 32 + 8 * q + 4 * (i ^ weh));
        riv[i] = *(const f32x4*)(stat_ri + wsn * 32 + 8 * q + 4 * (i ^ weh));
      }
#pragma unroll
      for (int jp = 0; jp < 4; ++jp) {
        const int j = jp ^ (2 * wsn);
        const int e6 = j * 16 + c;
        const int ii = ((e6 >> 5) << 1) | ((e6 >> 2) & 1), qq = (e6 >> 3) & 3, rr = e6 & 3;
        const int p = weh * 64 + 16 * ii + 4 * qq + rr;
        char* dst = (char*)abt + (size_t)((Qd1 * 4 + nt1) * 16 + ks1) * 16384 + p * 128;
        const int sw = (p >> 1) & 7;
        const float wsj = stat_ws[weh * 64 + j * 16 + c];
        f32x4 v0 = (acc[0][jp] - mbv[0] * wsj) * riv[0];
        f32x4 v1 = (acc[1][jp] - mbv[1] * wsj) * riv[1];
        const u32 a0 = pack_bf16(v0[0], v0[1]), a1 = pack_bf16(v0[2], v0[3]);
        const u32 b0 = pack_bf16(v1[0], v1[1]), b1 = pack_bf16(v1[2], v1[3]);
        u32x4 o0;
        o0[0] = weh ? b0 : a0; o0[1] = weh ? b1 : a1; o0[2] = weh ? a0 : b0; o0[3] = weh ? a1 : b1;
        st16_dev(dst + (((wsn * 4 + q) ^ sw) * 16), o0);
      }
      VM_DRAIN();
      if (l == 0) {
        const u32 n = __hip_atomic_fetch_add(misc + 2, 1u, __ATOMIC_RELAXED, __HIP_MEMORY_SCOPE_WORKGROUP);
        if (n == 3u) {
          st4_dev(ctl + CTL_FLAG1 + (size_t)((Qd1 * 4 + nt1) * 16 + ks1) * 128, ctag);
          __hip_atomic_store(misc + 4, 1u, __ATOMIC_RELAXED, __HIP_MEMORY_SCOPE_WORKGROUP);
        }
      }
    }
    {
      const int we = w & 1, wt = w >> 1;
      f32x4 acc[4][4] = {};
      const int sw = c >> 1;
      const int ro0 = c * 128 + ((q ^ sw) * 16);
      const int ro1 = c * 128 + (((q + 4) ^ sw) * 16);
      const int aoff = 16384 + we * 8192, boff = wt * 8192;
      bf16x8 a0[4], b0[4], a1[4], b1[4];
      auto rd = [&](bf16x8* af, bf16x8* bf, int buf, int ro) {
        const char* lb = smem + buf * P2_STG + ro;
#pragma unroll
        for (int i = 0; i < 4; ++i) { af[i] = *(const bf16x8*)(lb + aoff + i * 2048); bf[i] = *(const bf16x8*)(lb + boff + i * 2048); }
      };
      auto mm = [&](const bf16x8* af, const bf16x8* bf) {
#pragma unroll
        for (int i = 0; i < 4; ++i)
#pragma unroll
          for (int j = 0; j < 4; ++j)
            acc[i][j] = __builtin_amdgcn_mfma_f32_16x16x32_bf16(af[i], bf[j], acc[i][j], 0, 0, 0);
      };
      LDS_BARRIER();
      int buf = 0;
      rd(a0, b0, 0, ro0);
#pragma unroll 1
      for (int k = 0; k < 15; ++k) {
        rd(a1, b1, buf, ro1);
        mm(a0, b0);
        IL();
        SB();
        LDS_BARRIER();
        buf = (buf == 2) ? 0 : buf + 1;
        rd(a0, b0, buf, ro0);
        mm(a1, b1);
        IL();
        SB();
      }
      rd(a1, b1, buf, ro1);
      mm(a0, b0);
      IL();
      SB();
      mm(a1, b1);
#pragma unroll
      for (int j = 0; j < 4; ++j) {
        const int tr = wt * 64 + j * 16 + c;
        u32x4 o0, o1;
        o0[0] = pack_bf16(acc[0][j][0], acc[0][j][1]); o0[1] = pack_bf16(acc[0][j][2], acc[0][j][3]);
        o0[2] = pack_bf16(acc[1][j][0], acc[1][j][1]); o0[3] = pack_bf16(acc[1][j][2], acc[1][j][3]);
        o1[0] = pack_bf16(acc[2][j][0], acc[2][j][1]); o1[1] = pack_bf16(acc[2][j][2], acc[2][j][3]);
        o1[2] = pack_bf16(acc[3][j][0], acc[3][j][1]); o1[3] = pack_bf16(acc[3][j][2], acc[3][j][3]);
        *(u32x4*)(smem + P2_STG + tr * 256 + (((we * 8 + q) ^ c) * 16)) = o0;
        *(u32x4*)(smem + P2_STG + tr * 256 + (((we * 8 + 4 + q) ^ c) * 16)) = o1;
      }
    }
  }
  {
    LDS_BARRIER();
#pragma unroll
    for (int i = 0; i < 4; ++i) {
      const int tr = (w * 4 + i) * 4 + q;
      const u32x4 v = *(const u32x4*)(smem + P2_STG + tr * 256 + ((c ^ (tr & 15)) * 16));
      st16_dev(PQ + (size_t)Qd * PS + (size_t)(mtile * 128 + tr) * D_DIM + nt2 * 128 + c * 8, v);
    }
    VM_DRAIN();
    if (l == 0) {
      const u32 n = __hip_atomic_fetch_add(misc + 3, 1u, __ATOMIC_RELAXED, __HIP_MEMORY_SCOPE_WORKGROUP);
      if (n == 7u) {
        st4_dev(ctl + CTL_FLAG2 + (size_t)(mtile * 16 + Qd * 4 + nt2) * 128, misc[0]);
        __hip_atomic_store(misc + 6, 1u, __ATOMIC_RELAXED, __HIP_MEMORY_SCOPE_WORKGROUP);
      }
    }
  }

  const u32 tag = misc[0];
  const u32x4 wv = *(const u32x4*)(wr + l * 8);
  {
    const int row = xcd * 256 + slot * 8 + w;
    const size_t roff = (size_t)row * D_DIM + l * 8;
    const u32x4 xv = *(const u32x4*)(x + roff);
    if (w == 4) {
      while (__hip_atomic_load(misc + 6, __ATOMIC_RELAXED, __HIP_MEMORY_SCOPE_WORKGROUP) == 0u) __builtin_amdgcn_s_sleep(1);
      poll_words((const u32*)(ctl + CTL_FLAG2 + (size_t)((row >> 7) * 16 + c) * 128), tag);
    }
    wait_vm_bar<0>();
    u32x4 h, hn;
    row_h_hn(PQ + roff, xv, h, hn);
    float dot = 0.f;
#pragma unroll
    for (int j = 0; j < 4; ++j) dot = dot2bf(hn[j], wv[j], dot);
    dot = wave_sum(dot);
    if (l == 0) {
      const u32 u = pack_bf16(dot, 0.f) & 0xffffu;
      misc[8 + w] = (u & 0x8000u) ? (~u & 0xffffu) : (u | 0x8000u);
    }
    LDS_BARRIER();
    if (tid < 2) {
      u32x4 g;
      g[0] = tag;
      g[1] = misc[8 + tid * 4] | (misc[9 + tid * 4] << 16);
      g[2] = misc[10 + tid * 4] | (misc[11 + tid * 4] << 16);
      g[3] = tag;
      st16_dev(ctl + CTL_KEYS + (size_t)(xcd * 32 + slot) * 32 + tid * 16, g);
    }
    u32x4 o;
#pragma unroll
    for (int j = 0; j < 4; ++j) o[j] = pack_bf16(bf_lo(h[j]) + bf_lo(hn[j]), bf_hi(h[j]) + bf_hi(hn[j]));
    st16_dev(out + roff, o);
    VM_DRAIN();
    if (l == 0) {
      const u32 n = __hip_atomic_fetch_add(misc + 7, 1u, __ATOMIC_RELAXED, __HIP_MEMORY_SCOPE_WORKGROUP);
      if (n == 7u) st4_dev(ctl + CTL_FLAG3 + (size_t)(xcd * 32 + slot) * 128, tag);
    }
  }
  const int d0 = (xcd * 4 + (slot - 28)) * 16;
  if (is_p5) {
    const u16* src = wd + (size_t)(w * 224 + (l >> 1)) * D_DIM + d0 + (l & 1) * 8;
#pragma unroll
    for (int m = 0; m < 7; ++m)
      __builtin_amdgcn_global_load_lds(GLB_PTR(src + (size_t)m * 32 * D_DIM), LDS_PTR(smem + LDS_W + w * 7168 + m * 1024), 16, 0, 0);
  } else {
    const int f0 = (xcd * 28 + slot) * 8;
    const u16* src = ((l & 1) ? wu : wg) + (size_t)(w * 64 + (l >> 1)) * F_DIM + f0;
#pragma unroll
    for (int m = 0; m < 2; ++m)
      __builtin_amdgcn_global_load_lds(GLB_PTR(src + (size_t)m * 32 * F_DIM), LDS_PTR(smem + LDS_W + w * 2048 + m * 1024), 16, 0, 0);
  }

  u32* cand = (u32*)smem;
  u32* gmax = cand + 64;
  u32* cnt = cand + 72;
  u32* sel = cand + 80;
  char* red = smem + 4096;
  char* hnsel = smem + 16384;
  char* hsel = smem + 32768;
  u32 selkey;
  {
    if (tid < 64) cand[tid] = 0;
    if (tid == 0) *cnt = 0;
    u32x4 g;
#pragma unroll 1
    for (int spin = 0; spin < SPIN_CAP; ++spin) {
      g = ld16_dev_wait(ctl + CTL_KEYS + tid * 16);
      if (__all(g[0] == tag && g[3] == tag)) break;
      __builtin_amdgcn_s_sleep(2);
    }
    const u32 ib = (u32)(S_LEN - 1 - tid * 4);
    u32 a0 = (g[1] << 16) | ib, a1 = (g[1] & 0xffff0000u) | (ib - 1), a2 = (g[2] << 16) | (ib - 2), a3 = (g[2] & 0xffff0000u) | (ib - 3);
    {
      const u32 m = wave_max_u32(umax(umax(a0, a1), umax(a2, a3)));
      if (l == 0) gmax[w] = m;
    }
    LDS_BARRIER();
    {
      const u32x4 g0 = *(const u32x4*)(gmax), g1 = *(const u32x4*)(gmax + 4);
      const u32 T = umin(umin(umin(g0[0], g0[1]), umin(g0[2], g0[3])), umin(umin(g1[0], g1[1]), umin(g1[2], g1[3])));
      const u32 av[4] = {a0, a1, a2, a3};
      unsigned long long mk[4];
      u32 pre[4], tot = 0;
#pragma unroll
      for (int j = 0; j < 4; ++j) { mk[j] = __ballot(av[j] >= T); pre[j] = tot; tot += (u32)__builtin_popcountll(mk[j]); }
      u32 base = 0;
      if (l == 0) base = __hip_atomic_fetch_add(cnt, tot, __ATOMIC_RELAXED, __HIP_MEMORY_SCOPE_WORKGROUP);
      base = __builtin_amdgcn_readfirstlane(base);
#pragma unroll
      for (int j = 0; j < 4; ++j) {
        const u32 pos = base + pre[j] + __builtin_amdgcn_mbcnt_hi((u32)(mk[j] >> 32), __builtin_amdgcn_mbcnt_lo((u32)mk[j], 0u));
        if (av[j] >= T && pos < 64) cand[pos] = av[j];
      }
    }
    LDS_BARRIER();
    const u32 n = __builtin_amdgcn_readfirstlane(*cnt);
    if (n <= 64) {
      const u32 mine = cand[l];
      int rank = 0;
      for (u32 i0 = 0; i0 < n; i0 += 16) {
#pragma unroll
        for (int i = 0; i < 4; ++i) {
          const u32x4 v = *(const u32x4*)(cand + i0 + i * 4);
          rank += (v[0] > mine) + (v[1] > mine) + (v[2] > mine) + (v[3] > mine);
        }
      }
      if (rank < 8 && (u32)l < n) sel[w * 8 + rank] = mine;
      asm volatile("s_waitcnt lgkmcnt(0)" ::: "memory");
      selkey = sel[w * 8 + (l & 7)];
    } else {
      CE(a0, a1) CE(a2, a3) CE(a0, a2) CE(a1, a3) CE(a1, a2)
      u32 mycand = 0;
#pragma unroll 1
      for (int it = 0; it < 8; ++it) {
        const u32 m = wave_max_u32(a0);
        const bool win = (a0 == m);
        a0 = win ? a1 : a0; a1 = win ? a2 : a1; a2 = win ? a3 : a2; a3 = win ? 0u : a3;
        mycand = (l == it) ? m : mycand;
      }
      LDS_BARRIER();
      if (l < 8) cand[w * 8 + l] = mycand;
      LDS_BARRIER();
      const u32 mine = cand[l];
      int rank = 0;
#pragma unroll 1
      for (int i = 0; i < 16; ++i) {
        const u32x4 v = *(const u32x4*)(cand + i * 4);
        rank += (v[0] > mine) + (v[1] > mine) + (v[2] > mine) + (v[3] > mine);
      }
      if (rank < 8) sel[w * 8 + rank] = mine;
      asm volatile("s_waitcnt lgkmcnt(0)" ::: "memory");
      selkey = sel[w * 8 + (l & 7)];
    }
  }
  const int myrow = (S_LEN - 1 - (int)(selkey & 0xffffu)) & (S_LEN - 1);
  {
    const int trow = __builtin_amdgcn_readlane(myrow, w);
    const size_t roff = (size_t)trow * D_DIM + l * 8;
    const u32x4 xv = *(const u32x4*)(x + roff);
    u32x4 h, hn;
    row_h_hn(PQ + roff, xv, h, hn);
    *(u32x4*)(hnsel + w * 1040 + l * 16) = hn;
    *(u32x4*)(hsel + w * 1040 + l * 16) = h;
  }
  LDS_BARRIER();
  if (!is_p5) {
    const int ct = xcd * 28 + slot;
    f32x4 acc = {};
    {
      const char* base = smem + LDS_W + w * 2048 + q * 256 + c * 8;
#pragma unroll
      for (int ks = 0; ks < 2; ++ks) {
        const bf16x8 xf = *(const bf16x8*)(hnsel + (c & 7) * 1040 + w * 128 + ks * 64 + q * 16);
        s16x4 lo = __builtin_bit_cast(s16x4, __builtin_amdgcn_ds_read_tr16_b64_v4i16(
                       (__attribute__((address_space(3))) s16x4*)(base + ks * 1024)));
        s16x4 hi = __builtin_bit_cast(s16x4, __builtin_amdgcn_ds_read_tr16_b64_v4i16(
                       (__attribute__((address_space(3))) s16x4*)(base + ks * 1024 + 128)));
        bf16x8 a = {lo[0], lo[1], lo[2], lo[3], hi[0], hi[1], hi[2], hi[3]};
        acc = __builtin_amdgcn_mfma_f32_16x16x32_bf16(a, xf, acc, 0, 0, 0);
      }
    }
    *(f32x4*)(red + w * 1024 + l * 16) = acc;
    LDS_BARRIER();
    if (w == 0) {
      if (l < 32 && c < 8) {
        f32x4 g = *(const f32x4*)(red + l * 16);
        f32x4 u = *(const f32x4*)(red + (l + 32) * 16);
#pragma unroll
        for (int ww = 1; ww < 8; ++ww) {
          g += *(const f32x4*)(red + ww * 1024 + l * 16);
          u += *(const f32x4*)(red + ww * 1024 + (l + 32) * 16);
        }
        float o[4];
#pragma unroll
        for (int r = 0; r < 4; ++r) {
          const float gp = rbf(g[r]);
          const float sg = rbf(gp / (1.0f + __expf(-gp)));
          o[r] = sg * rbf(u[r]);
        }
        u32x4 ov = {tag, pack_bf16(o[0], o[1]), pack_bf16(o[2], o[3]), tag};
        st16_dev(ctl + CTL_GU + (size_t)ct * 256 + (q * 8 + c) * 16, ov);
      }
    }
    return;
  }

  {
    if (w == 0) poll_words((const u32*)(ctl + CTL_FLAG3 + (size_t)(myrow >> 3) * 128), tag);
    bf16x8 gf[7];
    {
      const char* src = ctl + CTL_GU + (size_t)(w * 28 + q) * 256 + c * 16;
#pragma unroll 1
      for (int spin = 0; spin < SPIN_CAP; ++spin) {
        const u32x4 g = ld16_dev_wait(src);
        if (__all(g[0] == tag && g[3] == tag)) break;
        __builtin_amdgcn_s_sleep(2);
      }
#pragma unroll 1
      for (int spin = 0; spin < SPIN_CAP; ++spin) {
        u32x4 g[7];
        asm volatile(
            "global_load_dwordx4 %0, %7, off sc1\n\tglobal_load_dwordx4 %1, %7, off offset:1024 sc1\n\t"
            "global_load_dwordx4 %2, %7, off offset:2048 sc1\n\tglobal_load_dwordx4 %3, %7, off offset:3072 sc1\n\t"
            "global_load_dwordx4 %4, %8, off sc1\n\tglobal_load_dwordx4 %5, %8, off offset:1024 sc1\n\t"
            "global_load_dwordx4 %6, %8, off offset:2048 sc1\n\ts_waitcnt vmcnt(0)"
            : "=&v"(g[0]), "=&v"(g[1]), "=&v"(g[2]), "=&v"(g[3]), "=&v"(g[4]), "=&v"(g[5]), "=&v"(g[6])
            : "v"(src), "v"(src + 4096) : "memory");
        bool ok = true;
#pragma unroll
        for (int i = 0; i < 7; ++i) ok = ok && (g[i][0] == tag) && (g[i][3] == tag);
#pragma unroll
        for (int ks = 0; ks < 7; ++ks) {
          const u32x4 f = {g[ks][1], g[ks][2], dppu<0x128>(g[ks][1]), dppu<0x128>(g[ks][2])};
          gf[ks] = __builtin_bit_cast(bf16x8, f);
        }
        if (__all(ok)) break;
        __builtin_amdgcn_s_sleep(1);
      }
    }
    f32x4 acc = {};
    {
      const char* base = smem + LDS_W + w * 7168 + q * 256 + c * 8;
#pragma unroll
      for (int ks = 0; ks < 7; ++ks) {
        s16x4 lo = __builtin_bit_cast(s16x4, __builtin_amdgcn_ds_read_tr16_b64_v4i16(
                       (__attribute__((address_space(3))) s16x4*)(base + ks * 1024)));
        s16x4 hi = __builtin_bit_cast(s16x4, __builtin_amdgcn_ds_read_tr16_b64_v4i16(
                       (__attribute__((address_space(3))) s16x4*)(base + ks * 1024 + 128)));
        bf16x8 a = {lo[0], lo[1], lo[2], lo[3], hi[0], hi[1], hi[2], hi[3]};
        acc = __builtin_amdgcn_mfma_f32_16x16x32_bf16(a, gf[ks], acc, 0, 0, 0);
      }
    }
    *(f32x4*)(red + w * 1024 + l * 16) = acc;
    LDS_BARRIER();
    if (b == 255 && tid == 64) st4_dev(ctl + CTL_EPOCH, tag);
    if (w == 0 && c < 8) {
      f32x4 y = *(const f32x4*)(red + l * 16);
#pragma unroll
      for (int ww = 1; ww < 8; ++ww) y += *(const f32x4*)(red + ww * 1024 + l * 16);
      const u32 key = selkey >> 16;
      const u32 u = (key & 0x8000u) ? (key & 0x7fffu) : (~key & 0xffffu);
      const float sg = rbf(1.0f / (1.0f + __expf(-__uint_as_float(u << 16))));
      const u32x2 hnv = *(const u32x2*)(hnsel + c * 1040 + (d0 + q * 4) * 2);
      const u32x2 hh = *(const u32x2*)(hsel + c * 1040 + (d0 + q * 4) * 2);
      float hn4[4] = {bf_lo(hnv[0]), bf_hi(hnv[0]), bf_lo(hnv[1]), bf_hi(hnv[1])};
      float h4[4] = {bf_lo(hh[0]), bf_hi(hh[0]), bf_lo(hh[1]), bf_hi(hh[1])};
      float o[4];
#pragma unroll
      for (int r = 0; r < 4; ++r) {
        const float yy = rbf(rbf(y[r]) * sg);
        o[r] = h4[r] + rbf(hn4[r] + yy);
      }
      u32x2 ov = {pack_bf16(o[0], o[1]), pack_bf16(o[2], o[3])};
      st8_dev(out + (size_t)myrow * D_DIM + d0 + q * 4, ov);
    }
    asm volatile("fnet_code_end:");
  }
}

extern "C" void kernel_launch(void* const* d_in, const int* in_sizes, int n_in, void* d_out, int out_size, void* d_ws,
                              size_t ws_size, hipStream_t stream) {
  fnet_moe_kernel<<<256, 512, 0, stream>>>((const u16*)d_in[0], (const u16*)d_in[1], (const u16*)d_in[2], (const u16*)d_in[3],
                                           (const u16*)d_in[4], (const u16*)d_in[6], (const u16*)d_in[7], (const u16*)d_in[8],
                                           (const u16*)d_in[9], (u16*)d_out, (char*)d_ws);
}
```
